```python
import torch

def _softmax(x, axis=-1):
    return torch.softmax(x, dim=axis)
def _gelu(x):
    return torch.nn.functional.gelu(x, approximate="tanh")
def _silu(x):
    return torch.nn.functional.silu(x)
def _sigmoid(x):
    return torch.sigmoid(x)
def _layernorm(x, axis=-1, eps=1e-5):
    m = x.mean(dim=axis, keepdim=True)
    v = x.var(dim=axis, keepdim=True, unbiased=False)
    return (x - m) / torch.sqrt(v + eps)
def _scatter_add(tgt, idx, src, axis=0):
    return tgt.clone().index_add_(axis, idx, src)
def _segment_sum(src, seg, n):
    out = torch.zeros((n,) + src.shape[1:], dtype=src.dtype)
    return out.index_add_(0, seg, src)
def _pad(x, pw):
    flat = tuple(v for pair in reversed(pw) for v in pair)
    return torch.nn.functional.pad(x, flat)

kernel_name = 'standalone__self_sup_conv_stem_wavlm__speech'

def setup_inputs():

    _w = (lambda a: (lambda t: t.to(torch.bfloat16) if t.is_floating_point() else t)(torch.as_tensor(a)))
    B, T, D, K = 2, 16000, 512, 3
    scale = (K * D) ** -0.5
    return {
        "x": _w((lambda *s: torch.randn(*s, dtype=torch.bfloat16) * 0.05)(B, T, D)),
        "w": _w((lambda *s: torch.randn(*s, dtype=torch.bfloat16) * 0.05)(K, D, D) * scale),
        "gn_gamma": _w((lambda *s: torch.randn(*s, dtype=torch.bfloat16) * 0.05)(D) * 0.1 + 1.0),
        "gn_beta": _w((lambda *s: torch.randn(*s, dtype=torch.bfloat16) * 0.05)(D) * 0.1),
    }

def reference(x, w, gn_gamma, gn_beta):
    B, T, D = x.shape
    K = w.shape[0]
    s = 2
    L = (T - K) // s + 1
    h = torch.einsum('blc,cd->bld', x[:, 0:s * L:s, :], w[0])
    for i in range(1, K):
        h = h + torch.einsum('blc,cd->bld', x[:, i:i + s * L:s, :], w[i])
    mu = torch.einsum('bld->bd', h)[:, None, :] / L
    var = torch.einsum('bld->bd', (h - mu) ** 2)[:, None, :] / L
    h = (h - mu) / torch.sqrt(var + 1e-5)
    h = h * gn_gamma[None, None, :] + gn_beta[None, None, :]
    return _gelu(h)
```

```cpp
#include <hip/hip_runtime.h>
#include <hip/hip_bf16.h>
#include <stdint.h>
constexpr int B=2,T=16000,D=512,L=7999;
__device__ inline float bf2f(unsigned short u){ return __uint_as_float(((unsigned)u)<<16); }
__global__ void k_gemm(const unsigned short* x, const unsigned short* w, float* h){
  __shared__ float xs[16][64];
  int b = blockIdx.y; int l0 = blockIdx.x*16; int d = threadIdx.x;
  float acc[16]; for(int i=0;i<16;i++) acc[i]=0.f;
  for(int k0=0;k0<1536;k0+=64){
    __syncthreads();
    for(int i=threadIdx.x;i<16*64;i+=512){ int r=i/64,c=i%64; int l=l0+r;
      xs[r][c] = (l<L)? bf2f(x[(size_t)b*T*D + (size_t)l*2*D + k0 + c]) : 0.f; }
    __syncthreads();
    for(int c=0;c<64;c++){ float wv = bf2f(w[(size_t)(k0+c)*D + d]);
      for(int r=0;r<16;r++) acc[r] += xs[r][c]*wv; }
  }
  for(int r=0;r<16;r++){ int l=l0+r; if(l<L) h[((size_t)b*L+l)*D+d]=acc[r]; }
}
__global__ void k_stats(const float* h, float* st){
  int b=blockIdx.y; int d=blockIdx.x*64+(threadIdx.x&63); int p=threadIdx.x>>6;
  __shared__ float red[16][64];
  float s=0; for(int l=p;l<L;l+=16) s+=h[((size_t)b*L+l)*D+d];
  red[p][threadIdx.x&63]=s; __syncthreads();
  float mu=0; for(int i=0;i<16;i++) mu+=red[i][threadIdx.x&63]; mu/=L; __syncthreads();
  float v=0; for(int l=p;l<L;l+=16){ float t=h[((size_t)b*L+l)*D+d]-mu; v+=t*t; }
  red[p][threadIdx.x&63]=v; __syncthreads();
  if(p==0){ float vv=0; for(int i=0;i<16;i++) vv+=red[i][threadIdx.x&63]; vv/=L; st[(b*D+d)*2]=mu; st[(b*D+d)*2+1]=rsqrtf(vv+1e-5f);}
}
__global__ void k_out(const float* h, const float* st, const unsigned short* g, const unsigned short* be, __hip_bfloat16* out){
  size_t i=(size_t)blockIdx.x*256+threadIdx.x; if(i>=(size_t)B*L*D) return;
  int d=i%D; int b=i/((size_t)L*D);
  float y=(h[i]-st[(b*D+d)*2])*st[(b*D+d)*2+1]*bf2f(g[d])+bf2f(be[d]);
  float u=0.7978845608f*(y+0.044715f*y*y*y);
  out[i]=__float2bfloat16(0.5f*y*(1.f+tanhf(u)));
}
extern "C" void kernel_launch(void* const* d_in, const int* in_sizes, int n_in, void* d_out, int out_size, void* d_ws, size_t ws_size, hipStream_t stream){
  const unsigned short* x=(const unsigned short*)d_in[0]; const unsigned short* w=(const unsigned short*)d_in[1];
  float* h=(float*)d_ws; float* st=h+(size_t)B*L*D;
  k_gemm<<<dim3((L+15)/16,B),512,0,stream>>>(x,w,h);
  k_stats<<<dim3(D/64,B),1024,0,stream>>>(h,st);
  k_out<<<(B*L*D+255)/256,256,0,stream>>>(h,st,(const unsigned short*)d_in[2],(const unsigned short*)d_in[3],(__hip_bfloat16*)d_out);
}
```

```cpp
#include <hip/hip_runtime.h>
#include <stdint.h>

__global__ __launch_bounds__(512, 2) void conv_stem_kernel(const void* x, const void* w, const void* gamma, const void* beta, void* out, void* ws) {
  __shared__ __attribute__((aligned(16))) char smem[163840];
  asm volatile(
      "s_load_dwordx8 s[4:11], s[0:1], 0\n\t"
      "s_load_dwordx4 s[12:15], s[0:1], 32\n\t"
      "s_getpc_b64 s[46:47]\n\t"
      "conv_pc1:\n\t"
      "v_mul_u32_u24 v37, 40, v0\n\t"
      "s_add_u32 s46, s46, 0x300\n\t"
      "v_min_u32 v37, conv_end-conv_pc1-0x300, v37\n\t"
      "s_addc_u32 s47, s47, 0\n\t"
      "global_load_dword v17, v37, s[46:47]\n\t"
      "v_readfirstlane_b32 s3, v0\n\t"
      "v_and_b32 v1, 63, v0\n\t"
      "s_and_b32 s16, s2, 7\n\t"
      "s_lshl_b32 s16, s16, 3\n\t"
      "s_lshr_b32 s17, s2, 5\n\t"
      "s_add_u32 s16, s16, s17\n\t"
      "s_bfe_u32 s18, s2, 0x20003\n\t"
      "s_lshr_b32 s19, s16, 5\n\t"
      "s_and_b32 s20, s16, 31\n\t"
      "s_lshr_b32 s3, s3, 6\n\t"
      "s_cmp_eq_u32 s20, 31\n\t"
      "s_subb_u32 s21, 250, 0\n\t"
      "s_cmp_ge_u32 s3, 4\n\t"
      "s_cbranch_scc1 conv_mfma\n\t"
      "s_lshl_b32 s16, s3, 3\n\t"
      "v_lshrrev_b32 v2, 3, v1\n\t"
      "v_add_u32 v2, s16, v2\n\t"
      "v_lshrrev_b32 v4, 1, v2\n\t"
      "v_xor_b32 v4, v4, v1\n\t"
      "v_and_b32 v4, 7, v4\n\t"
      "v_lshlrev_b32 v4, 4, v4\n\t"
      "v_lshl_or_b32 v2, v2, 14, v4\n\t"
      "s_lshl_b32 s16, s3, 1\n\t"
      "s_and_b32 s16, s16, 4\n\t"
      "v_lshrrev_b32 v3, 4, v1\n\t"
      "v_or_b32 v4, s16, v3\n\t"
      "v_lshlrev_b32 v4, 1, v4\n\t"
      "v_xor_b32 v4, v4, v1\n\t"
      "v_and_b32 v4, 15, v4\n\t"
      "v_lshlrev_b32 v4, 4, v4\n\t"
      "s_lshl_b32 s16, s3, 2\n\t"
      "v_add_u32 v3, s16, v3\n\t"
      "v_lshl_or_b32 v3, v3, 10, v4\n\t"
      "s_mul_i32 s16, s19, 16000\n\t"
      "s_mul_i32 s17, s20, 500\n\t"
      "s_add_u32 s16, s16, s17\n\t"
      "s_lshl_b32 s16, s16, 10\n\t"
      "s_lshl_b32 s26, s21, 11\n\t"
      "s_add_u32 s26, s26, 0x400\n\t"
      "s_mov_b32 s27, 0x20000\n\t"
      "s_lshl_b32 s17, s18, 8\n\t"
      "s_sub_u32 s30, 0x180000, s17\n\t"
      "s_mov_b32 s31, 0x20000\n\t"
      "s_lshl_b32 s38, s3, 10\n\t"
      "s_add_u32 s39, s38, 0x18000\n\t"
      "s_mov_b32 s55, 0\n\t"
      "s_mov_b32 s33, 0\n\t"
      "s_mov_b32 s34, 0\n\t"
      "s_mov_b32 s35, 0\n\t"
      "s_mov_b32 s36, 7\n\t"
      "s_and_b32 s46, s2, 7\n\t"
      "s_mov_b32 s58, 0x37654210\n\t"
      "s_bitcmp1_b32 0xaa, s46\n\t"
      "s_cselect_b32 s58, 0x76543210, s58\n\t"
      "s_mov_b32 s59, 0x32765410\n\t"
      "s_bitcmp1_b32 0x55, s46\n\t"
      "s_cselect_b32 s59, 0x76543210, s59\n\t"
      "s_and_b32 s35, s58, 7\n\t"
      "s_lshl_b32 s34, s35, 7\n\t"
      "s_lshl_b32 s35, s35, 16\n\t"
      "s_and_b32 s57, s59, 7\n\t"
      "s_lshl_b32 s56, s57, 7\n\t"
      "s_lshl_b32 s57, s57, 16\n\t"
      "s_waitcnt lgkmcnt(0)\n\t"
      "s_add_u32 s28, s6, s17\n\t"
      "s_addc_u32 s29, s7, 0\n\t"
      "s_add_u32 s24, s4, s16\n\t"
      "s_addc_u32 s25, s5, 0\n\t"
      "s_add_u32 m0, s38, s55\n\t"
      "s_add_u32 s37, s34, 0\n\t"
      "buffer_load_dwordx4 v2, s[24:27], s37 offen lds\n\t"
      "s_add_u32 m0, m0, 0x1000\n\t"
      "s_add_u32 s37, s37, 0x800\n\t"
      "buffer_load_dwordx4 v2, s[24:27], s37 offen lds\n\t"
      "s_add_u32 m0, m0, 0x1000\n\t"
      "s_add_u32 s37, s37, 0x800\n\t"
      "buffer_load_dwordx4 v2, s[24:27], s37 offen lds\n\t"
      "s_add_u32 m0, m0, 0x1000\n\t"
      "s_add_u32 s37, s37, 0x800\n\t"
      "buffer_load_dwordx4 v2, s[24:27], s37 offen lds\n\t"
      "s_add_u32 m0, m0, 0x1000\n\t"
      "s_add_u32 s37, s37, 0x800\n\t"
      "buffer_load_dwordx4 v2, s[24:27], s37 offen lds\n\t"
      "s_add_u32 m0, m0, 0x1000\n\t"
      "s_add_u32 s37, s37, 0x800\n\t"
      "buffer_load_dwordx4 v2, s[24:27], s37 offen lds\n\t"
      "s_add_u32 m0, m0, 0x1000\n\t"
      "s_add_u32 s37, s37, 0x800\n\t"
      "buffer_load_dwordx4 v2, s[24:27], s37 offen lds\n\t"
      "s_add_u32 m0, m0, 0x1000\n\t"
      "s_add_u32 s37, s37, 0x800\n\t"
      "buffer_load_dwordx4 v2, s[24:27], s37 offen lds\n\t"
      "s_add_u32 s55, s55, 0x8000\n\t"
      "s_cmp_eq_u32 s55, 0x18000\n\t"
      "s_cselect_b32 s55, 0, s55\n\t"
      "s_add_u32 m0, s39, s33\n\t"
      "s_add_u32 s37, s35, 0\n\t"
      "buffer_load_dwordx4 v3, s[28:31], s37 offen lds\n\t"
      "s_add_u32 m0, m0, 0x1000\n\t"
      "s_add_u32 s37, s35, 16384\n\t"
      "buffer_load_dwordx4 v3, s[28:31], s37 offen lds\n\t"
      "s_add_u32 m0, m0, 0x1000\n\t"
      "s_add_u32 s37, s35, 1048576\n\t"
      "buffer_load_dwordx4 v3, s[28:31], s37 offen lds\n\t"
      "s_add_u32 m0, m0, 0x1000\n\t"
      "s_add_u32 s37, s35, 1064960\n\t"
      "buffer_load_dwordx4 v3, s[28:31], s37 offen lds\n\t"
      "s_add_u32 s33, s33, 0x4000\n\t"
      "s_and_b32 s33, s33, 0xffff\n\t"
      "s_add_u32 m0, s39, s33\n\t"
      "s_add_u32 s37, s35, 32768\n\t"
      "buffer_load_dwordx4 v3, s[28:31], s37 offen lds\n\t"
      "s_add_u32 m0, m0, 0x1000\n\t"
      "s_add_u32 s37, s35, 49152\n\t"
      "buffer_load_dwordx4 v3, s[28:31], s37 offen lds\n\t"
      "s_add_u32 m0, m0, 0x1000\n\t"
      "s_add_u32 s37, s35, 1081344\n\t"
      "buffer_load_dwordx4 v3, s[28:31], s37 offen lds\n\t"
      "s_add_u32 m0, m0, 0x1000\n\t"
      "s_add_u32 s37, s35, 1097728\n\t"
      "buffer_load_dwordx4 v3, s[28:31], s37 offen lds\n\t"
      "s_add_u32 s33, s33, 0x4000\n\t"
      "s_and_b32 s33, s33, 0xffff\n\t"
      "s_add_u32 m0, s39, s33\n\t"
      "s_add_u32 s37, s57, 524288\n\t"
      "buffer_load_dwordx4 v3, s[28:31], s37 offen lds\n\t"
      "s_add_u32 m0, m0, 0x1000\n\t"
      "s_add_u32 s37, s57, 540672\n\t"
      "buffer_load_dwordx4 v3, s[28:31], s37 offen lds\n\t"
      "s_add_u32 m0, m0, 0x1000\n\t"
      "s_add_u32 s37, s57, 557056\n\t"
      "buffer_load_dwordx4 v3, s[28:31], s37 offen lds\n\t"
      "s_add_u32 m0, m0, 0x1000\n\t"
      "s_add_u32 s37, s57, 573440\n\t"
      "buffer_load_dwordx4 v3, s[28:31], s37 offen lds\n\t"
      "s_add_u32 s33, s33, 0x4000\n\t"
      "s_and_b32 s33, s33, 0xffff\n\t"
      "s_add_u32 m0, s38, s55\n\t"
      "s_add_u32 s37, s56, 1024\n\t"
      "buffer_load_dwordx4 v2, s[24:27], s37 offen lds\n\t"
      "s_add_u32 m0, m0, 0x1000\n\t"
      "s_add_u32 s37, s37, 0x800\n\t"
      "buffer_load_dwordx4 v2, s[24:27], s37 offen lds\n\t"
      "s_waitcnt vmcnt(10)\n\t"
      "s_barrier\n\t"
      "s_add_u32 m0, m0, 0x1000\n\t"
      "s_add_u32 s37, s37, 0x800\n\t"
      "buffer_load_dwordx4 v2, s[24:27], s37 offen lds\n\t"
      "s_add_u32 m0, m0, 0x1000\n\t"
      "s_add_u32 s37, s37, 0x800\n\t"
      "buffer_load_dwordx4 v2, s[24:27], s37 offen lds\n\t"
      "s_add_u32 m0, m0, 0x1000\n\t"
      "s_add_u32 s37, s37, 0x800\n\t"
      "buffer_load_dwordx4 v2, s[24:27], s37 offen lds\n\t"
      "s_add_u32 m0, m0, 0x1000\n\t"
      "s_add_u32 s37, s37, 0x800\n\t"
      "buffer_load_dwordx4 v2, s[24:27], s37 offen lds\n\t"
      "s_add_u32 m0, m0, 0x1000\n\t"
      "s_add_u32 s37, s37, 0x800\n\t"
      "buffer_load_dwordx4 v2, s[24:27], s37 offen lds\n\t"
      "s_add_u32 m0, m0, 0x1000\n\t"
      "s_add_u32 s37, s37, 0x800\n\t"
      "buffer_load_dwordx4 v2, s[24:27], s37 offen lds\n\t"
      "s_add_u32 s55, s55, 0x8000\n\t"
      "s_cmp_eq_u32 s55, 0x18000\n\t"
      "s_cselect_b32 s55, 0, s55\n\t"
      "s_lshr_b32 s58, s58, 4\n\t"
      "s_lshr_b32 s59, s59, 4\n\t"
      "s_and_b32 s35, s58, 7\n\t"
      "s_lshl_b32 s34, s35, 7\n\t"
      "s_lshl_b32 s35, s35, 16\n\t"
      "s_and_b32 s57, s59, 7\n\t"
      "s_lshl_b32 s56, s57, 7\n\t"
      "s_lshl_b32 s57, s57, 16\n\t"
      "s_add_u32 m0, s39, s33\n\t"
      "s_add_u32 s37, s35, 0\n\t"
      "buffer_load_dwordx4 v3, s[28:31], s37 offen lds\n\t"
      "s_add_u32 m0, m0, 0x1000\n\t"
      "s_add_u32 s37, s35, 16384\n\t"
      "buffer_load_dwordx4 v3, s[28:31], s37 offen lds\n\t"
      "s_waitcnt vmcnt(14)\n\t"
      "s_barrier\n\t"
      "s_add_u32 m0, m0, 0x1000\n\t"
      "s_add_u32 s37, s35, 1048576\n\t"
      "buffer_load_dwordx4 v3, s[28:31], s37 offen lds\n\t"
      "s_add_u32 m0, m0, 0x1000\n\t"
      "s_add_u32 s37, s35, 1064960\n\t"
      "buffer_load_dwordx4 v3, s[28:31], s37 offen lds\n\t"
      "s_add_u32 s33, s33, 0x4000\n\t"
      "s_and_b32 s33, s33, 0xffff\n\t"
      "s_add_u32 m0, s38, s55\n\t"
      "s_add_u32 s37, s34, 0\n\t"
      "buffer_load_dwordx4 v2, s[24:27], s37 offen lds\n\t"
      "s_add_u32 m0, m0, 0x1000\n\t"
      "s_add_u32 s37, s37, 0x800\n\t"
      "buffer_load_dwordx4 v2, s[24:27], s37 offen lds\n\t"
      "s_add_u32 m0, m0, 0x1000\n\t"
      "s_add_u32 s37, s37, 0x800\n\t"
      "buffer_load_dwordx4 v2, s[24:27], s37 offen lds\n\t"
      "s_add_u32 m0, m0, 0x1000\n\t"
      "s_add_u32 s37, s37, 0x800\n\t"
      "buffer_load_dwordx4 v2, s[24:27], s37 offen lds\n\t"
      "s_add_u32 m0, m0, 0x1000\n\t"
      "s_add_u32 s37, s37, 0x800\n\t"
      "buffer_load_dwordx4 v2, s[24:27], s37 offen lds\n\t"
      "s_add_u32 m0, m0, 0x1000\n\t"
      "s_add_u32 s37, s37, 0x800\n\t"
      "buffer_load_dwordx4 v2, s[24:27], s37 offen lds\n\t"
      "s_branch conv_ld_b1\n\t"
      "conv_ld_loop:\n\t"
      "s_add_u32 m0, s39, s33\n\t"
      "s_add_u32 s37, s35, 0\n\t"
      "buffer_load_dwordx4 v3, s[28:31], s37 offen lds\n\t"
      "s_add_u32 m0, m0, 0x1000\n\t"
      "s_add_u32 s37, s35, 16384\n\t"
      "buffer_load_dwordx4 v3, s[28:31], s37 offen lds\n\t"
      "s_add_u32 m0, m0, 0x1000\n\t"
      "s_add_u32 s37, s35, 1048576\n\t"
      "buffer_load_dwordx4 v3, s[28:31], s37 offen lds\n\t"
      "s_add_u32 m0, m0, 0x1000\n\t"
      "s_add_u32 s37, s35, 1064960\n\t"
      "buffer_load_dwordx4 v3, s[28:31], s37 offen lds\n\t"
      "s_add_u32 s33, s33, 0x4000\n\t"
      "s_and_b32 s33, s33, 0xffff\n\t"
      "s_add_u32 m0, s38, s55\n\t"
      "s_add_u32 s37, s34, 0\n\t"
      "buffer_load_dwordx4 v2, s[24:27], s37 offen lds\n\t"
      "s_add_u32 m0, m0, 0x1000\n\t"
      "s_add_u32 s37, s37, 0x800\n\t"
      "buffer_load_dwordx4 v2, s[24:27], s37 offen lds\n\t"
      "s_add_u32 m0, m0, 0x1000\n\t"
      "s_add_u32 s37, s37, 0x800\n\t"
      "buffer_load_dwordx4 v2, s[24:27], s37 offen lds\n\t"
      "s_add_u32 m0, m0, 0x1000\n\t"
      "s_add_u32 s37, s37, 0x800\n\t"
      "buffer_load_dwordx4 v2, s[24:27], s37 offen lds\n\t"
      "s_add_u32 m0, m0, 0x1000\n\t"
      "s_add_u32 s37, s37, 0x800\n\t"
      "buffer_load_dwordx4 v2, s[24:27], s37 offen lds\n\t"
      "s_add_u32 m0, m0, 0x1000\n\t"
      "s_add_u32 s37, s37, 0x800\n\t"
      "buffer_load_dwordx4 v2, s[24:27], s37 offen lds\n\t"
      "conv_ld_in:\n\t"
      "s_waitcnt vmcnt(22)\n\t"
      "s_barrier\n\t"
      "conv_ld_b1:\n\t"
      "s_add_u32 m0, s38, s55\n\t"
      "s_add_u32 s37, s34, 12288\n\t"
      "s_add_u32 m0, m0, 24576\n\t"
      "s_nop 0\n\t"
      "buffer_load_dwordx4 v2, s[24:27], s37 offen lds\n\t"
      "s_add_u32 m0, m0, 0x1000\n\t"
      "s_add_u32 s37, s37, 0x800\n\t"
      "buffer_load_dwordx4 v2, s[24:27], s37 offen lds\n\t"
      "s_add_u32 s55, s55, 0x8000\n\t"
      "s_cmp_eq_u32 s55, 0x18000\n\t"
      "s_cselect_b32 s55, 0, s55\n\t"
      "s_add_u32 m0, s39, s33\n\t"
      "s_add_u32 s37, s35, 32768\n\t"
      "buffer_load_dwordx4 v3, s[28:31], s37 offen lds\n\t"
      "s_add_u32 m0, m0, 0x1000\n\t"
      "s_add_u32 s37, s35, 49152\n\t"
      "buffer_load_dwordx4 v3, s[28:31], s37 offen lds\n\t"
      "s_add_u32 m0, m0, 0x1000\n\t"
      "s_add_u32 s37, s35, 1081344\n\t"
      "buffer_load_dwordx4 v3, s[28:31], s37 offen lds\n\t"
      "s_add_u32 m0, m0, 0x1000\n\t"
      "s_add_u32 s37, s35, 1097728\n\t"
      "buffer_load_dwordx4 v3, s[28:31], s37 offen lds\n\t"
      "s_add_u32 s33, s33, 0x4000\n\t"
      "s_and_b32 s33, s33, 0xffff\n\t"
      "s_cmp_eq_u32 s36, 2\n\t"
      "s_cbranch_scc0 conv_nopf\n\t"
      "s_getpc_b64 s[46:47]\n\t"
      "conv_pc0:\n\t"
      "v_lshlrev_b32 v5, 6, v0\n\t"
      "v_cmp_lt_u32 vcc, conv_mfma-conv_pc0, v5\n\t"
      "v_add_u32 v4, conv_tail-conv_mfma-64, v5\n\t"
      "s_nop 0\n\t"
      "v_cndmask_b32 v5, v5, v4, vcc\n\t"
      "v_min_u32 v5, conv_end-conv_pc0, v5\n\t"
      "global_load_dword v5, v5, s[46:47]\n\t"
      "s_lshl_b32 s16, s19, 2\n\t"
      "s_add_u32 s16, s16, s18\n\t"
      "s_lshl_b32 s17, s16, 8\n\t"
      "s_add_u32 s40, s14, s17\n\t"
      "s_addc_u32 s41, s15, 0\n\t"
      "s_lshl_b32 s17, s16, 11\n\t"
      "s_add_u32 s17, s17, 0x800\n\t"
      "s_add_u32 s42, s14, s17\n\t"
      "s_addc_u32 s43, s15, 0\n\t"
      "s_lshl_b32 s17, s16, 15\n\t"
      "s_add_u32 s17, s17, 0x8000\n\t"
      "s_add_u32 s44, s14, s17\n\t"
      "s_addc_u32 s45, s15, 0\n\t"
      "s_and_b32 s16, s3, 1\n\t"
      "s_lshl_b32 s16, s16, 6\n\t"
      "s_lshr_b32 s17, s3, 1\n\t"
      "s_lshl_b32 s17, s17, 5\n\t"
      "s_add_u32 s54, s16, s17\n\t"
      "v_and_b32 v6, 15, v1\n\t"
      "v_lshl_add_u32 v6, v6, 1, s54\n\t"
      "v_bfe_u32 v4, v1, 4, 1\n\t"
      "v_add_u32 v4, v4, v6\n\t"
      "s_lshl_b32 s16, s18, 8\n\t"
      "v_lshl_add_u32 v8, v4, 1, s16\n\t"
      "v_and_b32 v35, 31, v1\n\t"
      "v_mov_b32 v20, 0\n\t"
      "v_lshlrev_b32 v27, 2, v35\n\t"
      "v_lshlrev_b32 v35, 6, v35\n\t"
      "conv_nopf:\n\t"
      "s_waitcnt vmcnt(16)\n\t"
      "s_barrier\n\t"
      "s_add_u32 m0, s39, s33\n\t"
      "s_add_u32 s37, s57, 524288\n\t"
      "buffer_load_dwordx4 v3, s[28:31], s37 offen lds\n\t"
      "s_add_u32 m0, m0, 0x1000\n\t"
      "s_add_u32 s37, s57, 540672\n\t"
      "buffer_load_dwordx4 v3, s[28:31], s37 offen lds\n\t"
      "s_add_u32 m0, m0, 0x1000\n\t"
      "s_add_u32 s37, s57, 557056\n\t"
      "buffer_load_dwordx4 v3, s[28:31], s37 offen lds\n\t"
      "s_add_u32 m0, m0, 0x1000\n\t"
      "s_add_u32 s37, s57, 573440\n\t"
      "buffer_load_dwordx4 v3, s[28:31], s37 offen lds\n\t"
      "s_add_u32 s33, s33, 0x4000\n\t"
      "s_and_b32 s33, s33, 0xffff\n\t"
      "s_add_u32 m0, s38, s55\n\t"
      "s_add_u32 s37, s56, 1024\n\t"
      "buffer_load_dwordx4 v2, s[24:27], s37 offen lds\n\t"
      "s_add_u32 m0, m0, 0x1000\n\t"
      "s_add_u32 s37, s37, 0x800\n\t"
      "buffer_load_dwordx4 v2, s[24:27], s37 offen lds\n\t"
      "s_add_u32 m0, m0, 0x1000\n\t"
      "s_add_u32 s37, s37, 0x800\n\t"
      "buffer_load_dwordx4 v2, s[24:27], s37 offen lds\n\t"
      "s_add_u32 m0, m0, 0x1000\n\t"
      "s_add_u32 s37, s37, 0x800\n\t"
      "buffer_load_dwordx4 v2, s[24:27], s37 offen lds\n\t"
      "s_add_u32 m0, m0, 0x1000\n\t"
      "s_add_u32 s37, s37, 0x800\n\t"
      "buffer_load_dwordx4 v2, s[24:27], s37 offen lds\n\t"
      "s_add_u32 m0, m0, 0x1000\n\t"
      "s_add_u32 s37, s37, 0x800\n\t"
      "buffer_load_dwordx4 v2, s[24:27], s37 offen lds\n\t"
      "s_add_u32 m0, m0, 0x1000\n\t"
      "s_add_u32 s37, s37, 0x800\n\t"
      "buffer_load_dwordx4 v2, s[24:27], s37 offen lds\n\t"
      "s_add_u32 m0, m0, 0x1000\n\t"
      "s_add_u32 s37, s37, 0x800\n\t"
      "buffer_load_dwordx4 v2, s[24:27], s37 offen lds\n\t"
      "s_add_u32 s55, s55, 0x8000\n\t"
      "s_cmp_eq_u32 s55, 0x18000\n\t"
      "s_cselect_b32 s55, 0, s55\n\t"
      "s_lshr_b32 s58, s58, 4\n\t"
      "s_lshr_b32 s59, s59, 4\n\t"
      "s_and_b32 s35, s58, 7\n\t"
      "s_lshl_b32 s34, s35, 7\n\t"
      "s_lshl_b32 s35, s35, 16\n\t"
      "s_and_b32 s57, s59, 7\n\t"
      "s_lshl_b32 s56, s57, 7\n\t"
      "s_lshl_b32 s57, s57, 16\n\t"
      "s_sub_u32 s36, s36, 1\n\t"
      "s_cmp_lg_u32 s36, 0\n\t"
      "s_waitcnt vmcnt(16)\n\t"
      "s_barrier\n\t"
      "s_cbranch_scc1 conv_ld_loop\n\t"
      "ds_write_b32 v27, v20 offset:38912\n\t"
      "global_load_ushort v9, v8, s[10:11]\n\t"
      "global_load_ushort v8, v8, s[8:9]\n\t"
      "global_load_dwordx2 v[12:13], v35, s[42:43] sc1\n\t"
      "s_waitcnt vmcnt(15) lgkmcnt(0)\n\t"
      "s_barrier\n\t"
      "s_waitcnt vmcnt(3)\n\t"
      "s_barrier\n\t"
      "s_setprio 3\n\t"
      "ds_read_b64 v[10:11], v20 offset:39168\n\t"
      "v_lshlrev_b32 v26, 3, v6\n\t"
      "v_lshrrev_b32 v21, 4, v1\n\t"
      "v_lshl_add_u32 v21, v21, 10, v26\n\t"
      "v_add_u32 v21, 0x1000, v21\n\t"
      "v_add_u32 v22, 0x2000, v21\n\t"
      "v_add_u32 v23, 0x4000, v21\n\t"
      "v_add_u32 v24, 0x6000, v21\n\t"
      "v_lshlrev_b32 v25, 2, v6\n\t"
      "v_add_u32 v25, 32768, v25\n\t"
      "v_lshlrev_b32 v32, 2, v4\n\t"
      "v_add_u32 v32, 32768, v32\n\t"
      "s_lshr_b32 s16, s3, 1\n\t"
      "s_lshl_b32 s16, s16, 1\n\t"
      "s_and_b32 s17, s3, 1\n\t"
      "s_add_u32 s16, s16, s17\n\t"
      "s_lshl_b32 s17, s16, 3\n\t"
      "s_lshl_b32 s16, s16, 2\n\t"
      "s_add_u32 s17, s17, 38912\n\t"
      "s_add_u32 s16, s16, 38976\n\t"
      "v_mov_b32 v27, s17\n\t"
      "v_mov_b32 v33, s16\n\t"
      "v_mov_b32 v34, 1\n\t"
      "s_mov_b32 s48, 0x3c064b8a\n\t"
      "s_mov_b32 s49, 0x3ef9db23\n\t"
      "s_mov_b32 s50, 0x4279db23\n\t"
      "s_cmp_eq_u32 s20, 31\n\t"
      "s_cselect_b32 s48, 0x3c0767ab, s48\n\t"
      "s_cselect_b32 s49, 0x3ef8cd9f, s49\n\t"
      "s_cselect_b32 s50, 0x4278cd9f, s50\n\t"
      "v_mov_b32 v44, 0x437a0000\n\t"
      "v_mov_b32 v45, 0x437a0000\n\t"
      "v_mov_b64 v[46:47], 0\n\t"
      "s_lshl_b32 s16, s20, 10\n\t"
      "s_add_u32 s52, s44, s16\n\t"
      "s_addc_u32 s53, s45, 0\n\t"
      "s_mov_b32 s51, 0\n\t"
      "s_cmp_lt_u32 s3, 2\n\t"
      "s_cbranch_scc1 conv_part\n\t"
      "s_sleep 48\n\t"
      "conv_part:\n\t"
      "s_sleep 1\n\t"
      "ds_read_b64 v[30:31], v27\n\t"
      "ds_read2st64_b64 v[48:51], v25 offset0:2 offset1:3\n\t"
      "ds_read2st64_b64 v[52:55], v25 offset0:4 offset1:5\n\t"
      "ds_read2st64_b64 v[14:17], v25 offset1:1\n\t"
      "s_waitcnt lgkmcnt(3)\n\t"
      "v_and_b32 v30, v30, v31\n\t"
      "v_cmp_eq_u32 vcc, 0, v30\n\t"
      "s_waitcnt lgkmcnt(0)\n\t"
      "s_cbranch_vccnz conv_part\n\t"
      "v_mul_f32 v56, 0x3c000000, v48\n\t"
      "v_mul_f32 v57, 0x3c000000, v49\n\t"
      "v_mul_f32 v58, s48, v50\n\t"
      "v_mul_f32 v59, s48, v51\n\t"
      "v_sub_f32 v16, v16, v14\n\t"
      "v_sub_f32 v17, v17, v15\n\t"
      "v_fma_f32 v52, -v48, v56, v52\n\t"
      "v_fma_f32 v53, -v49, v57, v53\n\t"
      "v_fma_f32 v54, -v50, v58, v54\n\t"
      "v_fma_f32 v55, -v51, v59, v55\n\t"
      "v_sub_f32 v58, v58, v56\n\t"
      "v_sub_f32 v59, v59, v57\n\t"
      "v_add_f32 v52, v52, v54\n\t"
      "v_add_f32 v53, v53, v55\n\t"
      "v_add_f32 v58, v58, v16\n\t"
      "v_add_f32 v59, v59, v17\n\t"
      "v_fmac_f32 v56, s49, v58\n\t"
      "v_fmac_f32 v57, s49, v59\n\t"
      "v_mul_f32 v16, s50, v58\n\t"
      "v_mul_f32 v17, s50, v59\n\t"
      "v_add_f32 v28, v14, v56\n\t"
      "v_add_f32 v29, v15, v57\n\t"
      "v_fma_f32 v30, v16, v58, v52\n\t"
      "v_fma_f32 v31, v17, v59, v53\n\t"
      "v_min_u32 v29, -2, v29\n\t"
      "v_min_u32 v31, -2, v31\n\t"
      "s_mov_b64 exec, 0xffff\n\t"
      "global_store_dwordx4 v26, v[28:31], s[52:53] sc1\n\t"
      "s_mov_b64 exec, -1\n\t"
      "s_cmp_lt_u32 s3, 2\n\t"
      "s_cbranch_scc1 conv_dzA\n\t"
      "s_sleep 16\n\t"
      "s_branch conv_dzB\n\t"
      "conv_dzA:\n\t"
      "s_sleep 16\n\t"
      "conv_dzB:\n\t"
      "s_waitcnt vmcnt(1)\n\t"
      "v_lshlrev_b32 v8, 16, v8\n\t"
      "v_lshlrev_b32 v9, 16, v9\n\t"
      "v_mul_f32 v8, 0x3eeff558, v8\n\t"
      "v_mul_f32 v9, 0x3eeff558, v9\n\t"
      "conv_flag:\n\t"
      "v_cmp_ne_u64 vcc, v[12:13], v[10:11]\n\t"
      "s_cbranch_vccz conv_flag_ok\n\t"
      "s_add_u32 s51, s51, 1\n\t"
      "s_cmp_ge_u32 s51, 0x40000\n\t"
      "s_cbranch_scc1 conv_flag_ok\n\t"
      "s_sleep 1\n\t"
      "global_load_dwordx2 v[12:13], v35, s[42:43] sc1\n\t"
      "s_waitcnt vmcnt(0)\n\t"
      "s_branch conv_flag\n\t"
      "conv_flag_ok:\n\t"
      "global_load_dwordx4 v[64:67], v21, s[44:45] offset:-4096 sc1\n\t"
      "global_load_dwordx4 v[68:71], v21, s[44:45] offset:0 sc1\n\t"
      "global_load_dwordx4 v[72:75], v22, s[44:45] offset:-4096 sc1\n\t"
      "global_load_dwordx4 v[76:79], v22, s[44:45] offset:0 sc1\n\t"
      "global_load_dwordx4 v[80:83], v23, s[44:45] offset:-4096 sc1\n\t"
      "global_load_dwordx4 v[84:87], v23, s[44:45] offset:0 sc1\n\t"
      "global_load_dwordx4 v[88:91], v24, s[44:45] offset:-4096 sc1\n\t"
      "global_load_dwordx4 v[92:95], v24, s[44:45] offset:0 sc1\n\t"
      "conv_poll:\n\t"
      "s_waitcnt vmcnt(0)\n\t"
      "v_max3_u32 v96, v65, v67, v69\n\t"
      "v_max3_u32 v97, v73, v75, v77\n\t"
      "v_max3_u32 v98, v81, v83, v85\n\t"
      "v_max3_u32 v99, v89, v91, v93\n\t"
      "v_max_u32 v96, v96, v71\n\t"
      "v_max_u32 v97, v97, v79\n\t"
      "v_max_u32 v98, v98, v87\n\t"
      "v_max_u32 v99, v99, v95\n\t"
      "v_cmp_eq_u32 s[56:57], -1, v96\n\t"
      "v_cmp_eq_u32 s[58:59], -1, v97\n\t"
      "v_cmp_eq_u32 s[60:61], -1, v98\n\t"
      "v_cmp_eq_u32 s[62:63], -1, v99\n\t"
      "s_or_b64 s[16:17], s[56:57], s[58:59]\n\t"
      "s_or_b64 vcc, s[60:61], s[62:63]\n\t"
      "s_or_b64 vcc, vcc, s[16:17]\n\t"
      "s_cbranch_scc0 conv_poll_ok\n\t"
      "s_add_u32 s51, s51, 1\n\t"
      "s_cmp_ge_u32 s51, 0x40000\n\t"
      "s_cbranch_scc1 conv_poll_ok\n\t"
      "s_cmp_lg_u64 s[56:57], 0\n\t"
      "s_cbranch_scc0 conv_skip0\n\t"
      "global_load_dwordx4 v[64:67], v21, s[44:45] offset:-4096 sc1\n\t"
      "global_load_dwordx4 v[68:71], v21, s[44:45] offset:0 sc1\n\t"
      "conv_skip0:\n\t"
      "s_cmp_lg_u64 s[58:59], 0\n\t"
      "s_cbranch_scc0 conv_skip1\n\t"
      "global_load_dwordx4 v[72:75], v22, s[44:45] offset:-4096 sc1\n\t"
      "global_load_dwordx4 v[76:79], v22, s[44:45] offset:0 sc1\n\t"
      "conv_skip1:\n\t"
      "s_cmp_lg_u64 s[60:61], 0\n\t"
      "s_cbranch_scc0 conv_skip2\n\t"
      "global_load_dwordx4 v[80:83], v23, s[44:45] offset:-4096 sc1\n\t"
      "global_load_dwordx4 v[84:87], v23, s[44:45] offset:0 sc1\n\t"
      "conv_skip2:\n\t"
      "s_cmp_lg_u64 s[62:63], 0\n\t"
      "s_cbranch_scc0 conv_skip3\n\t"
      "global_load_dwordx4 v[88:91], v24, s[44:45] offset:-4096 sc1\n\t"
      "global_load_dwordx4 v[92:95], v24, s[44:45] offset:0 sc1\n\t"
      "conv_skip3:\n\t"
      "s_branch conv_poll\n\t"
      "conv_poll_ok:\n\t"
      "v_pk_add_f32 v[64:65], v[64:65], v[28:29] neg_lo:[0,1] neg_hi:[0,1]\n\t"
      "v_pk_add_f32 v[68:69], v[68:69], v[28:29] neg_lo:[0,1] neg_hi:[0,1]\n\t"
      "v_pk_add_f32 v[72:73], v[72:73], v[28:29] neg_lo:[0,1] neg_hi:[0,1]\n\t"
      "v_pk_add_f32 v[76:77], v[76:77], v[28:29] neg_lo:[0,1] neg_hi:[0,1]\n\t"
      "v_pk_add_f32 v[80:81], v[80:81], v[28:29] neg_lo:[0,1] neg_hi:[0,1]\n\t"
      "v_pk_add_f32 v[84:85], v[84:85], v[28:29] neg_lo:[0,1] neg_hi:[0,1]\n\t"
      "v_pk_add_f32 v[88:89], v[88:89], v[28:29] neg_lo:[0,1] neg_hi:[0,1]\n\t"
      "v_pk_add_f32 v[92:93], v[92:93], v[28:29] neg_lo:[0,1] neg_hi:[0,1]\n\t"
      "v_pk_mul_f32 v[48:49], v[64:65], v[64:65]\n\t"
      "v_pk_mul_f32 v[50:51], v[68:69], v[68:69]\n\t"
      "v_pk_fma_f32 v[48:49], v[72:73], v[72:73], v[48:49]\n\t"
      "v_pk_fma_f32 v[50:51], v[76:77], v[76:77], v[50:51]\n\t"
      "v_pk_fma_f32 v[48:49], v[80:81], v[80:81], v[48:49]\n\t"
      "v_pk_fma_f32 v[50:51], v[84:85], v[84:85], v[50:51]\n\t"
      "v_pk_fma_f32 v[48:49], v[88:89], v[88:89], v[48:49]\n\t"
      "v_pk_fma_f32 v[50:51], v[92:93], v[92:93], v[50:51]\n\t"
      "s_mov_b32 exec_lo, 0\n\t"
      "s_mov_b32 exec_hi, 0xffff0000\n\t"
      "v_mov_b64 v[46:47], v[92:93]\n\t"
      "s_mov_b64 exec, -1\n\t"
      "v_pk_add_f32 v[64:65], v[64:65], v[80:81]\n\t"
      "v_pk_add_f32 v[66:67], v[66:67], v[82:83]\n\t"
      "v_pk_add_f32 v[68:69], v[68:69], v[84:85]\n\t"
      "v_pk_add_f32 v[70:71], v[70:71], v[86:87]\n\t"
      "v_pk_add_f32 v[72:73], v[72:73], v[88:89]\n\t"
      "v_pk_add_f32 v[74:75], v[74:75], v[90:91]\n\t"
      "v_pk_add_f32 v[76:77], v[76:77], v[92:93]\n\t"
      "v_pk_add_f32 v[78:79], v[78:79], v[94:95]\n\t"
      "v_pk_add_f32 v[64:65], v[64:65], v[72:73]\n\t"
      "v_pk_add_f32 v[66:67], v[66:67], v[74:75]\n\t"
      "v_pk_add_f32 v[68:69], v[68:69], v[76:77]\n\t"
      "v_pk_add_f32 v[70:71], v[70:71], v[78:79]\n\t"
      "v_pk_add_f32 v[64:65], v[64:65], v[68:69]\n\t"
      "v_pk_add_f32 v[66:67], v[66:67], v[70:71]\n\t"
      "v_pk_add_f32 v[48:49], v[48:49], v[50:51]\n\t"
      "v_pk_fma_f32 v[64:65], v[64:65], v[44:45], v[46:47] neg_lo:[0,0,1] neg_hi:[0,0,1]\n\t"
      "v_pk_fma_f32 v[66:67], v[48:49], v[44:45], v[66:67]\n\t"
      "v_pk_fma_f32 v[66:67], v[46:47], v[46:47], v[66:67] neg_lo:[1,0,0] neg_hi:[1,0,0]\n\t"
      "s_mov_b32 vcc_lo, 0xffff0000\n\t"
      "s_mov_b32 vcc_hi, 0xffff0000\n\t"
      "v_permlane16_swap_b32 v64, v65\n\t"
      "v_permlane16_swap_b32 v66, v67\n\t"
      "v_cndmask_b32 v28, v28, v29, vcc\n\t"
      "v_add_f32 v64, v64, v65\n\t"
      "v_add_f32 v66, v66, v67\n\t"
      "s_nop 1\n\t"
      "v_permlane32_swap_b32 v64, v66\n\t"
      "v_add_f32 v64, v64, v66\n\t"
      "v_mov_b32 v65, v64\n\t"
      "s_nop 1\n\t"
      "v_permlane32_swap_b32 v64, v65\n\t"
      "v_mul_f32 v66, 0x390316a0, v64\n\t"
      "v_fma_f32 v65, -v64, v66, v65\n\t"
      "v_mov_b32 v67, 0x3727c5ac\n\t"
      "v_fmac_f32 v67, 0x390316a0, v65\n\t"
      "v_rsq_f32 v67, v67\n\t"
      "v_add_f32 v28, v28, v66\n\t"
      "s_cmp_ge_u32 s51, 0x40000\n\t"
      "s_cselect_b32 s16, 0x7fc00000, 1.0\n\t"
      "v_mul_f32 v67, v67, v8\n\t"
      "v_mul_f32 v67, s16, v67\n\t"
      "v_fma_f32 v28, -v28, v67, v9\n\t"
      "ds_write2st64_b32 v32, v67, v28 offset0:16 offset1:18\n\t"
      "ds_write_b32 v33, v34\n\t"
      "s_or_b32 s16, s20, s3\n\t"
      "s_cmp_lg_u32 s16, 0\n\t"
      "s_cbranch_scc1 conv_ld_done\n\t"
      "s_mov_b64 exec, 1\n\t"
      "global_store_dwordx2 v20, v[10:11], s[40:41] sc1\n\t"
      "conv_ld_done:\n\t"
      "s_endpgm\n\t"
      ".p2align 6\n\t"
      "conv_mfma:\n\t"
      "s_setprio 1\n\t"
      "s_bfe_u32 s22, s3, 0x10001\n\t"
      "s_and_b32 s23, s3, 1\n\t"
      "s_lshl_b32 s16, s22, 4\n\t"
      "v_and_b32 v2, 15, v1\n\t"
      "v_lshrrev_b32 v18, 2, v2\n\t"
      "v_add_u32 v2, s16, v2\n\t"
      "v_lshrrev_b32 v3, 4, v1\n\t"
      "v_lshrrev_b32 v8, 1, v2\n\t"
      "v_and_b32 v8, 7, v8\n\t"
      "v_xor_b32 v8, v8, v3\n\t"
      "v_lshlrev_b32 v8, 4, v8\n\t"
      "v_lshl_or_b32 v8, v2, 7, v8\n\t"
      "v_xor_b32 v9, 64, v8\n\t"
      "v_add_u32 v11, 1, v2\n\t"
      "v_lshrrev_b32 v10, 1, v11\n\t"
      "v_and_b32 v10, 7, v10\n\t"
      "v_xor_b32 v10, v10, v3\n\t"
      "v_lshlrev_b32 v10, 4, v10\n\t"
      "v_lshl_or_b32 v10, v11, 7, v10\n\t"
      "v_xor_b32 v11, 64, v10\n\t"
      "v_and_b32 v19, 1, v3\n\t"
      "v_lshl_or_b32 v20, v19, 2, v18\n\t"
      "v_lshl_add_u32 v21, v3, 3, v18\n\t"
      "v_lshlrev_b32 v21, 8, v21\n\t"
      "v_and_b32 v22, 1, v1\n\t"
      "v_lshl_or_b32 v21, v22, 4, v21\n\t"
      "v_add_u32 v21, 0x18000, v21\n\t"
      "v_bfe_u32 v22, v1, 1, 1\n\t"
      "s_lshl_b32 s16, s23, 2\n\t"
      "v_or_b32 v22, s16, v22\n\t"
      "v_xor_b32 v20, v22, v20\n\t"
      "v_lshl_add_u32 v4, v20, 5, v21\n\t"
      "v_xor_b32 v20, 2, v20\n\t"
      "v_lshl_add_u32 v5, v20, 5, v21\n\t"
      "s_lshl_b32 s16, s23, 6\n\t"
      "v_lshl_add_u32 v22, v3, 3, s16\n\t"
      "s_lshl_b32 s16, s22, 9\n\t"
      "s_add_u32 s16, s16, 32768\n\t"
      "v_lshl_add_u32 v12, v22, 2, s16\n\t"
      "v_lshlrev_b32 v14, 2, v22\n\t"
      "v_add_u32 v14, 36864, v14\n\t"
      "v_bfe_u32 v23, v1, 3, 1\n\t"
      "v_add_u32 v22, v22, v23\n\t"
      "v_bfe_u32 v23, v1, 2, 1\n\t"
      "v_lshl_add_u32 v22, v23, 1, v22\n\t"
      "s_lshl_b32 s16, s22, 9\n\t"
      "s_add_u32 s16, s16, 33792\n\t"
      "v_lshl_add_u32 v13, v22, 2, s16\n\t"
      "v_lshlrev_b32 v15, 13, v2\n\t"
      "v_lshl_or_b32 v15, v3, 4, v15\n\t"
      "v_lshlrev_b32 v16, 4, v1\n\t"
      "s_lshl_b32 s16, s23, 1\n\t"
      "s_add_u32 s16, s16, s22\n\t"
      "s_lshl_b32 s16, s16, 2\n\t"
      "s_add_u32 s16, s16, 38912\n\t"
      "v_mov_b32 v6, s16\n\t"
      "s_lshl_b32 s16, s23, 2\n\t"
      "s_add_u32 s16, s16, 38976\n\t"
      "v_mov_b32 v7, s16\n\t"
      "v_mov_b32 v2, 1\n\t"
      "v_mov_b32 v34, 0xc09d30fc\n\t"
      "v_mov_b32 v35, v34\n\t"
      "v_mov_b32 v36, 0x3eeff558\n\t"
      "v_mov_b32 v37, v36\n\t"
      "s_cmp_lg_u32 s22, 0\n\t"
      "s_cselect_b32 s58, 0x7fff7fff, -1\n\t"
      "s_mov_b32 s59, s58\n\t"
      "s_cmp_eq_u32 s20, 31\n\t"
      "s_cselect_b32 s56, s58, -1\n\t"
      "s_mov_b32 s57, s56\n\t"
      "s_mul_i32 s24, s19, 7999\n\t"
      "s_mul_i32 s25, s20, 250\n\t"
      "s_add_u32 s24, s24, s25\n\t"
      "s_lshl_b32 s24, s24, 10\n\t"
      "s_lshl_b32 s25, s18, 8\n\t"
      "s_add_u32 s24, s24, s25\n\t"
      "s_lshl_b32 s25, s23, 7\n\t"
      "s_add_u32 s24, s24, s25\n\t"
      "s_lshl_b32 s26, s21, 10\n\t"
      "s_sub_u32 s26, s26, 896\n\t"
      "s_mov_b32 s27, 0x20000\n\t"
      "s_mov_b32 s28, 0x1000\n\t"
      "s_cmp_eq_u32 s3, 7\n\t"
      "s_cselect_b32 s35, 7, 0xff\n\t"
      "s_waitcnt lgkmcnt(0)\n\t"
      "s_add_u32 s24, s12, s24\n\t"
      "s_addc_u32 s25, s13, 0\n\t"
      "s_cmp_eq_u32 s3, 7\n\t"
      "s_cbranch_scc0 2f\n\t"
      "s_lshl_b32 s16, s19, 2\n\t"
      "s_add_u32 s16, s16, s18\n\t"
      "s_lshl_b32 s17, s16, 8\n\t"
      "s_add_u32 s40, s14, s17\n\t"
      "s_addc_u32 s41, s15, 0\n\t"
      "s_lshl_b32 s17, s16, 11\n\t"
      "s_add_u32 s17, s17, 0x800\n\t"
      "s_add_u32 s42, s14, s17\n\t"
      "s_addc_u32 s43, s15, 0\n\t"
      "s_lshl_b32 s17, s16, 15\n\t"
      "s_add_u32 s17, s17, 0x8000\n\t"
      "s_add_u32 s44, s14, s17\n\t"
      "s_addc_u32 s45, s15, 0\n\t"
      "s_lshl_b32 s16, s20, 10\n\t"
      "s_add_u32 s44, s44, s16\n\t"
      "s_addc_u32 s45, s45, 0\n\t"
      "s_lshl_b32 s16, s20, 6\n\t"
      "s_add_u32 s42, s42, s16\n\t"
      "s_addc_u32 s43, s43, 0\n\t"
      "v_mov_b32 v40, 0\n\t"
      "v_mov_b64 v[44:45], -1\n\t"
      "v_mov_b64 v[46:47], -1\n\t"
      "global_load_dwordx2 v[38:39], v40, s[40:41] sc1\n\t"
      "global_store_dwordx4 v16, v[44:47], s[44:45] sc1\n\t"
      "s_nop 1\n\t"
      "s_branch 2f\n\t"
      "3:\n\t"
      "s_mov_b32 s35, 0xff\n\t"
      "s_waitcnt vmcnt(0)\n\t"
      "v_lshl_add_u64 v[38:39], v[38:39], 0, 1\n\t"
      "s_mov_b64 exec, 1\n\t"
      "global_store_dwordx2 v40, v[38:39], s[42:43] sc1\n\t"
      "s_mov_b64 exec, -1\n\t"
      "s_branch 4f\n\t"
      "2:\n\t"
      "v_mov_b32 v43, v4\n\t"
      "v_mov_b32 v42, v8\n\t"
      "v_mov_b32 v41, v10\n\t"
      "s_mov_b32 s36, 0\n\t"
      "s_mov_b32 s33, 0\n\t"
      "s_mov_b32 s34, 7\n\t"
      "v_mov_b64 v[128:129], 0\n\t"
      "v_mov_b64 v[130:131], 0\n\t"
      "v_mov_b64 v[132:133], 0\n\t"
      "v_mov_b64 v[134:135], 0\n\t"
      "v_mov_b64 v[136:137], 0\n\t"
      "v_mov_b64 v[138:139], 0\n\t"
      "v_mov_b64 v[140:141], 0\n\t"
      "v_mov_b64 v[142:143], 0\n\t"
      "v_mov_b64 v[144:145], 0\n\t"
      "v_mov_b64 v[146:147], 0\n\t"
      "v_mov_b64 v[148:149], 0\n\t"
      "v_mov_b64 v[150:151], 0\n\t"
      "v_mov_b64 v[152:153], 0\n\t"
      "v_mov_b64 v[154:155], 0\n\t"
      "v_mov_b64 v[156:157], 0\n\t"
      "v_mov_b64 v[158:159], 0\n\t"
      "v_mov_b64 v[160:161], 0\n\t"
      "v_mov_b64 v[162:163], 0\n\t"
      "v_mov_b64 v[164:165], 0\n\t"
      "v_mov_b64 v[166:167], 0\n\t"
      "v_mov_b64 v[168:169], 0\n\t"
      "v_mov_b64 v[170:171], 0\n\t"
      "v_mov_b64 v[172:173], 0\n\t"
      "v_mov_b64 v[174:175], 0\n\t"
      "v_mov_b64 v[176:177], 0\n\t"
      "v_mov_b64 v[178:179], 0\n\t"
      "v_mov_b64 v[180:181], 0\n\t"
      "v_mov_b64 v[182:183], 0\n\t"
      "v_mov_b64 v[184:185], 0\n\t"
      "v_mov_b64 v[186:187], 0\n\t"
      "v_mov_b64 v[188:189], 0\n\t"
      "v_mov_b64 v[190:191], 0\n\t"
      "v_mov_b64 v[192:193], 0\n\t"
      "v_mov_b64 v[194:195], 0\n\t"
      "v_mov_b64 v[196:197], 0\n\t"
      "v_mov_b64 v[198:199], 0\n\t"
      "v_mov_b64 v[200:201], 0\n\t"
      "v_mov_b64 v[202:203], 0\n\t"
      "v_mov_b64 v[204:205], 0\n\t"
      "v_mov_b64 v[206:207], 0\n\t"
      "v_mov_b64 v[208:209], 0\n\t"
      "v_mov_b64 v[210:211], 0\n\t"
      "v_mov_b64 v[212:213], 0\n\t"
      "v_mov_b64 v[214:215], 0\n\t"
      "v_mov_b64 v[216:217], 0\n\t"
      "v_mov_b64 v[218:219], 0\n\t"
      "v_mov_b64 v[220:221], 0\n\t"
      "v_mov_b64 v[222:223], 0\n\t"
      "v_mov_b64 v[224:225], 0\n\t"
      "v_mov_b64 v[226:227], 0\n\t"
      "v_mov_b64 v[228:229], 0\n\t"
      "v_mov_b64 v[230:231], 0\n\t"
      "v_mov_b64 v[232:233], 0\n\t"
      "v_mov_b64 v[234:235], 0\n\t"
      "v_mov_b64 v[236:237], 0\n\t"
      "v_mov_b64 v[238:239], 0\n\t"
      "v_mov_b64 v[240:241], 0\n\t"
      "v_mov_b64 v[242:243], 0\n\t"
      "v_mov_b64 v[244:245], 0\n\t"
      "v_mov_b64 v[246:247], 0\n\t"
      "v_mov_b64 v[248:249], 0\n\t"
      "v_mov_b64 v[250:251], 0\n\t"
      "v_mov_b64 v[252:253], 0\n\t"
      "v_mov_b64 v[254:255], 0\n\t"
      "conv_dry_done:\n\t"
      "s_barrier\n\t"
      "ds_read_b64_tr_b16 v[44:45], v43 offset:0\n\t"
      "ds_read_b64_tr_b16 v[46:47], v43 offset:1024\n\t"
      "ds_read_b128 v[60:63], v42 offset:0\n\t"
      "ds_read_b64_tr_b16 v[48:49], v43 offset:8192\n\t"
      "ds_read_b64_tr_b16 v[50:51], v43 offset:9216\n\t"
      "ds_read_b128 v[64:67], v42 offset:4096\n\t"
      "ds_read_b128 v[68:71], v42 offset:8192\n\t"
      "ds_read_b128 v[72:75], v42 offset:12288\n\t"
      "ds_read_b128 v[76:79], v42 offset:16384\n\t"
      "ds_read_b128 v[80:83], v42 offset:20480\n\t"
      "ds_read_b128 v[84:87], v42 offset:24576\n\t"
      "ds_read_b128 v[88:91], v42 offset:28672\n\t"
      "ds_read_b128 v[92:95], v41\n\t"
      "s_branch conv_e0\n\t"
      ".p2align 6\n\t"
      "conv_loop:\n\t"
      "s_barrier\n\t"
      "conv_seg1:\n\t"
      "v_mfma_f32_16x16x32_bf16 v[140:143], v[52:55], v[64:67], v[140:143]\n\t"
      "ds_read_b64_tr_b16 v[44:45], v43 offset:0\n\t"
      "v_mfma_f32_16x16x32_bf16 v[140:143], v[56:59], v[68:71], v[140:143]\n\t"
      "ds_read_b64_tr_b16 v[46:47], v43 offset:1024\n\t"
      "v_mfma_f32_16x16x32_bf16 v[156:159], v[56:59], v[72:75], v[156:159]\n\t"
      "ds_read_b128 v[60:63], v42 offset:0\n\t"
      "v_mfma_f32_16x16x32_bf16 v[156:159], v[52:55], v[76:79], v[156:159]\n\t"
      "ds_read_b64_tr_b16 v[48:49], v43 offset:8192\n\t"
      "v_mfma_f32_16x16x32_bf16 v[172:175], v[52:55], v[80:83], v[172:175]\n\t"
      "ds_read_b64_tr_b16 v[50:51], v43 offset:9216\n\t"
      "v_mfma_f32_16x16x32_bf16 v[172:175], v[56:59], v[84:87], v[172:175]\n\t"
      "ds_read_b128 v[64:67], v42 offset:4096\n\t"
      "v_mfma_f32_16x16x32_bf16 v[188:191], v[56:59], v[88:91], v[188:191]\n\t"
      "ds_read_b128 v[68:71], v42 offset:8192\n\t"
      "v_mfma_f32_16x16x32_bf16 v[188:191], v[52:55], v[92:95], v[188:191]\n\t"
      "ds_read_b128 v[72:75], v42 offset:12288\n\t"
      "v_mfma_f32_16x16x32_bf16 v[204:207], v[52:55], v[96:99], v[204:207]\n\t"
      "ds_read_b128 v[76:79], v42 offset:16384\n\t"
      "v_mfma_f32_16x16x32_bf16 v[204:207], v[56:59], v[100:103], v[204:207]\n\t"
      "ds_read_b128 v[80:83], v42 offset:20480\n\t"
      "v_mfma_f32_16x16x32_bf16 v[220:223], v[56:59], v[104:107], v[220:223]\n\t"
      "ds_read_b128 v[84:87], v42 offset:24576\n\t"
      "v_mfma_f32_16x16x32_bf16 v[220:223], v[52:55], v[108:111], v[220:223]\n\t"
      "ds_read_b128 v[88:91], v42 offset:28672\n\t"
      "v_mfma_f32_16x16x32_bf16 v[236:239], v[52:55], v[112:115], v[236:239]\n\t"
      "ds_read_b128 v[92:95], v41\n\t"
      "v_mfma_f32_16x16x32_bf16 v[236:239], v[56:59], v[116:119], v[236:239]\n\t"
      "v_mfma_f32_16x16x32_bf16 v[252:255], v[56:59], v[120:123], v[252:255]\n\t"
      "v_mfma_f32_16x16x32_bf16 v[252:255], v[52:55], v[124:127], v[252:255]\n\t"
      "conv_e0:\n\t"
      "s_waitcnt lgkmcnt(7)\n\t"
      "v_mfma_f32_16x16x32_bf16 v[128:131], v[44:47], v[60:63], v[128:131]\n\t"
      "ds_read_b64_tr_b16 v[52:53], v43 offset:8\n\t"
      "v_mfma_f32_16x16x32_bf16 v[128:131], v[48:51], v[64:67], v[128:131]\n\t"
      "ds_read_b64_tr_b16 v[54:55], v43 offset:1032\n\t"
      "s_waitcnt lgkmcnt(8)\n\t"
      "v_mfma_f32_16x16x32_bf16 v[144:147], v[44:47], v[64:67], v[144:147]\n\t"
      "ds_read_b64_tr_b16 v[56:57], v43 offset:8200\n\t"
      "v_mfma_f32_16x16x32_bf16 v[144:147], v[48:51], v[68:71], v[144:147]\n\t"
      "ds_read_b64_tr_b16 v[58:59], v43 offset:9224\n\t"
      "s_waitcnt lgkmcnt(9)\n\t"
      "v_mfma_f32_16x16x32_bf16 v[160:163], v[44:47], v[68:71], v[160:163]\n\t"
      "v_mfma_f32_16x16x32_bf16 v[160:163], v[48:51], v[72:75], v[160:163]\n\t"
      "s_waitcnt lgkmcnt(8)\n\t"
      "v_mfma_f32_16x16x32_bf16 v[176:179], v[44:47], v[72:75], v[176:179]\n\t"
      "v_mfma_f32_16x16x32_bf16 v[176:179], v[48:51], v[76:79], v[176:179]\n\t"
      "s_waitcnt lgkmcnt(7)\n\t"
      "v_mfma_f32_16x16x32_bf16 v[192:195], v[44:47], v[76:79], v[192:195]\n\t"
      "v_mfma_f32_16x16x32_bf16 v[192:195], v[48:51], v[80:83], v[192:195]\n\t"
      "v_add_u32 v43, s36, v5\n\t"
      "s_waitcnt lgkmcnt(6)\n\t"
      "v_mfma_f32_16x16x32_bf16 v[208:211], v[44:47], v[80:83], v[208:211]\n\t"
      "v_mfma_f32_16x16x32_bf16 v[208:211], v[48:51], v[84:87], v[208:211]\n\t"
      "s_waitcnt lgkmcnt(5)\n\t"
      "v_mfma_f32_16x16x32_bf16 v[224:227], v[44:47], v[84:87], v[224:227]\n\t"
      "v_mfma_f32_16x16x32_bf16 v[224:227], v[48:51], v[88:91], v[224:227]\n\t"
      "s_waitcnt lgkmcnt(4)\n\t"
      "v_mfma_f32_16x16x32_bf16 v[240:243], v[44:47], v[88:91], v[240:243]\n\t"
      "v_mfma_f32_16x16x32_bf16 v[240:243], v[48:51], v[92:95], v[240:243]\n\t"
      "s_waitcnt lgkmcnt(0)\n\t"
      "v_mfma_f32_16x16x32_bf16 v[132:135], v[52:55], v[60:63], v[132:135]\n\t"
      "ds_read_b64_tr_b16 v[44:45], v43 offset:0\n\t"
      "v_mfma_f32_16x16x32_bf16 v[132:135], v[56:59], v[64:67], v[132:135]\n\t"
      "ds_read_b64_tr_b16 v[46:47], v43 offset:1024\n\t"
      "v_mfma_f32_16x16x32_bf16 v[148:151], v[52:55], v[64:67], v[148:151]\n\t"
      "ds_read_b64_tr_b16 v[48:49], v43 offset:8192\n\t"
      "v_mfma_f32_16x16x32_bf16 v[148:151], v[56:59], v[68:71], v[148:151]\n\t"
      "ds_read_b64_tr_b16 v[50:51], v43 offset:9216\n\t"
      "v_mfma_f32_16x16x32_bf16 v[164:167], v[52:55], v[68:71], v[164:167]\n\t"
      "v_mfma_f32_16x16x32_bf16 v[164:167], v[56:59], v[72:75], v[164:167]\n\t"
      "v_mfma_f32_16x16x32_bf16 v[180:183], v[52:55], v[72:75], v[180:183]\n\t"
      "v_mfma_f32_16x16x32_bf16 v[180:183], v[56:59], v[76:79], v[180:183]\n\t"
      "v_mfma_f32_16x16x32_bf16 v[196:199], v[52:55], v[76:79], v[196:199]\n\t"
      "v_mfma_f32_16x16x32_bf16 v[196:199], v[56:59], v[80:83], v[196:199]\n\t"
      "v_mfma_f32_16x16x32_bf16 v[212:215], v[52:55], v[80:83], v[212:215]\n\t"
      "v_mfma_f32_16x16x32_bf16 v[212:215], v[56:59], v[84:87], v[212:215]\n\t"
      "v_mfma_f32_16x16x32_bf16 v[228:231], v[52:55], v[84:87], v[228:231]\n\t"
      "v_mfma_f32_16x16x32_bf16 v[228:231], v[56:59], v[88:91], v[228:231]\n\t"
      "v_mfma_f32_16x16x32_bf16 v[244:247], v[52:55], v[88:91], v[244:247]\n\t"
      "v_mfma_f32_16x16x32_bf16 v[244:247], v[56:59], v[92:95], v[244:247]\n\t"
      "s_waitcnt lgkmcnt(0)\n\t"
      "v_mfma_f32_16x16x32_bf16 v[136:139], v[44:47], v[60:63], v[136:139]\n\t"
      "ds_read_b64_tr_b16 v[52:53], v43 offset:8\n\t"
      "v_mfma_f32_16x16x32_bf16 v[136:139], v[48:51], v[64:67], v[136:139]\n\t"
      "ds_read_b64_tr_b16 v[54:55], v43 offset:1032\n\t"
      "v_mfma_f32_16x16x32_bf16 v[152:155], v[44:47], v[64:67], v[152:155]\n\t"
      "ds_read_b64_tr_b16 v[56:57], v43 offset:8200\n\t"
      "v_mfma_f32_16x16x32_bf16 v[152:155], v[48:51], v[68:71], v[152:155]\n\t"
      "ds_read_b64_tr_b16 v[58:59], v43 offset:9224\n\t"
      "v_mfma_f32_16x16x32_bf16 v[168:171], v[44:47], v[68:71], v[168:171]\n\t"
      "v_mfma_f32_16x16x32_bf16 v[168:171], v[48:51], v[72:75], v[168:171]\n\t"
      "s_add_u32 s36, s36, 0x4000\n\t"
      "v_mfma_f32_16x16x32_bf16 v[184:187], v[44:47], v[72:75], v[184:187]\n\t"
      "s_and_b32 s36, s36, 0xffff\n\t"
      "v_mfma_f32_16x16x32_bf16 v[184:187], v[48:51], v[76:79], v[184:187]\n\t"
      "v_add_u32 v43, s36, v4\n\t"
      "v_mfma_f32_16x16x32_bf16 v[200:203], v[44:47], v[76:79], v[200:203]\n\t"
      "v_mfma_f32_16x16x32_bf16 v[200:203], v[48:51], v[80:83], v[200:203]\n\t"
      "v_mfma_f32_16x16x32_bf16 v[216:219], v[44:47], v[80:83], v[216:219]\n\t"
      "v_mfma_f32_16x16x32_bf16 v[216:219], v[48:51], v[84:87], v[216:219]\n\t"
      "v_add_u32 v42, s33, v9\n\t"
      "v_mfma_f32_16x16x32_bf16 v[232:235], v[44:47], v[84:87], v[232:235]\n\t"
      "v_add_u32 v41, s33, v11\n\t"
      "v_mfma_f32_16x16x32_bf16 v[232:235], v[48:51], v[88:91], v[232:235]\n\t"
      "v_mfma_f32_16x16x32_bf16 v[248:251], v[44:47], v[88:91], v[248:251]\n\t"
      "s_waitcnt lgkmcnt(0)\n\t"
      "v_mfma_f32_16x16x32_bf16 v[248:251], v[48:51], v[92:95], v[248:251]\n\t"
      "s_barrier\n\t"
      "conv_seg2:\n\t"
      "v_mfma_f32_16x16x32_bf16 v[140:143], v[52:55], v[60:63], v[140:143]\n\t"
      "ds_read_b64_tr_b16 v[44:45], v43 offset:0\n\t"
      "v_mfma_f32_16x16x32_bf16 v[140:143], v[56:59], v[64:67], v[140:143]\n\t"
      "ds_read_b64_tr_b16 v[46:47], v43 offset:1024\n\t"
      "v_mfma_f32_16x16x32_bf16 v[156:159], v[52:55], v[64:67], v[156:159]\n\t"
      "ds_read_b128 v[96:99], v42 offset:0\n\t"
      "v_mfma_f32_16x16x32_bf16 v[156:159], v[56:59], v[68:71], v[156:159]\n\t"
      "ds_read_b64_tr_b16 v[48:49], v43 offset:8192\n\t"
      "v_mfma_f32_16x16x32_bf16 v[172:175], v[52:55], v[68:71], v[172:175]\n\t"
      "ds_read_b64_tr_b16 v[50:51], v43 offset:9216\n\t"
      "v_mfma_f32_16x16x32_bf16 v[172:175], v[56:59], v[72:75], v[172:175]\n\t"
      "ds_read_b128 v[100:103], v42 offset:4096\n\t"
      "v_mfma_f32_16x16x32_bf16 v[188:191], v[52:55], v[72:75], v[188:191]\n\t"
      "ds_read_b128 v[104:107], v42 offset:8192\n\t"
      "v_mfma_f32_16x16x32_bf16 v[188:191], v[56:59], v[76:79], v[188:191]\n\t"
      "ds_read_b128 v[108:111], v42 offset:12288\n\t"
      "v_mfma_f32_16x16x32_bf16 v[204:207], v[52:55], v[76:79], v[204:207]\n\t"
      "ds_read_b128 v[112:115], v42 offset:16384\n\t"
      "v_mfma_f32_16x16x32_bf16 v[204:207], v[56:59], v[80:83], v[204:207]\n\t"
      "ds_read_b128 v[116:119], v42 offset:20480\n\t"
      "v_mfma_f32_16x16x32_bf16 v[220:223], v[52:55], v[80:83], v[220:223]\n\t"
      "ds_read_b128 v[120:123], v42 offset:24576\n\t"
      "v_mfma_f32_16x16x32_bf16 v[220:223], v[56:59], v[84:87], v[220:223]\n\t"
      "ds_read_b128 v[124:127], v42 offset:28672\n\t"
      "v_mfma_f32_16x16x32_bf16 v[236:239], v[52:55], v[84:87], v[236:239]\n\t"
      "ds_read_b128 v[60:63], v41\n\t"
      "v_mfma_f32_16x16x32_bf16 v[236:239], v[56:59], v[88:91], v[236:239]\n\t"
      "v_mfma_f32_16x16x32_bf16 v[252:255], v[52:55], v[88:91], v[252:255]\n\t"
      "v_mfma_f32_16x16x32_bf16 v[252:255], v[56:59], v[92:95], v[252:255]\n\t"
      "conv_e1:\n\t"
      "s_waitcnt lgkmcnt(7)\n\t"
      "v_mfma_f32_16x16x32_bf16 v[128:131], v[44:47], v[96:99], v[128:131]\n\t"
      "ds_read_b64_tr_b16 v[52:53], v43 offset:8\n\t"
      "v_mfma_f32_16x16x32_bf16 v[128:131], v[48:51], v[100:103], v[128:131]\n\t"
      "ds_read_b64_tr_b16 v[54:55], v43 offset:1032\n\t"
      "s_waitcnt lgkmcnt(8)\n\t"
      "v_mfma_f32_16x16x32_bf16 v[144:147], v[44:47], v[100:103], v[144:147]\n\t"
      "ds_read_b64_tr_b16 v[56:57], v43 offset:8200\n\t"
      "v_mfma_f32_16x16x32_bf16 v[144:147], v[48:51], v[104:107], v[144:147]\n\t"
      "ds_read_b64_tr_b16 v[58:59], v43 offset:9224\n\t"
      "s_waitcnt lgkmcnt(9)\n\t"
      "v_mfma_f32_16x16x32_bf16 v[160:163], v[44:47], v[104:107], v[160:163]\n\t"
      "v_mfma_f32_16x16x32_bf16 v[160:163], v[48:51], v[108:111], v[160:163]\n\t"
      "s_waitcnt lgkmcnt(8)\n\t"
      "v_mfma_f32_16x16x32_bf16 v[176:179], v[44:47], v[108:111], v[176:179]\n\t"
      "v_mfma_f32_16x16x32_bf16 v[176:179], v[48:51], v[112:115], v[176:179]\n\t"
      "s_waitcnt lgkmcnt(7)\n\t"
      "v_mfma_f32_16x16x32_bf16 v[192:195], v[44:47], v[112:115], v[192:195]\n\t"
      "v_mfma_f32_16x16x32_bf16 v[192:195], v[48:51], v[116:119], v[192:195]\n\t"
      "v_add_u32 v43, s36, v5\n\t"
      "s_waitcnt lgkmcnt(6)\n\t"
      "v_mfma_f32_16x16x32_bf16 v[208:211], v[44:47], v[116:119], v[208:211]\n\t"
      "v_mfma_f32_16x16x32_bf16 v[208:211], v[48:51], v[120:123], v[208:211]\n\t"
      "s_waitcnt lgkmcnt(5)\n\t"
      "v_mfma_f32_16x16x32_bf16 v[224:227], v[44:47], v[120:123], v[224:227]\n\t"
      "v_mfma_f32_16x16x32_bf16 v[224:227], v[48:51], v[124:127], v[224:227]\n\t"
      "s_waitcnt lgkmcnt(4)\n\t"
      "v_mfma_f32_16x16x32_bf16 v[240:243], v[44:47], v[124:127], v[240:243]\n\t"
      "v_mfma_f32_16x16x32_bf16 v[240:243], v[48:51], v[60:63], v[240:243]\n\t"
      "s_waitcnt lgkmcnt(0)\n\t"
      "v_mfma_f32_16x16x32_bf16 v[132:135], v[52:55], v[96:99], v[132:135]\n\t"
      "ds_read_b64_tr_b16 v[44:45], v43 offset:0\n\t"
      "v_mfma_f32_16x16x32_bf16 v[132:135], v[56:59], v[100:103], v[132:135]\n\t"
      "ds_read_b64_tr_b16 v[46:47], v43 offset:1024\n\t"
      "v_mfma_f32_16x16x32_bf16 v[148:151], v[52:55], v[100:103], v[148:151]\n\t"
      "ds_read_b64_tr_b16 v[48:49], v43 offset:8192\n\t"
      "v_mfma_f32_16x16x32_bf16 v[148:151], v[56:59], v[104:107], v[148:151]\n\t"
      "ds_read_b64_tr_b16 v[50:51], v43 offset:9216\n\t"
      "v_mfma_f32_16x16x32_bf16 v[164:167], v[52:55], v[104:107], v[164:167]\n\t"
      "v_mfma_f32_16x16x32_bf16 v[164:167], v[56:59], v[108:111], v[164:167]\n\t"
      "v_mfma_f32_16x16x32_bf16 v[180:183], v[52:55], v[108:111], v[180:183]\n\t"
      "v_mfma_f32_16x16x32_bf16 v[180:183], v[56:59], v[112:115], v[180:183]\n\t"
      "v_mfma_f32_16x16x32_bf16 v[196:199], v[52:55], v[112:115], v[196:199]\n\t"
      "v_mfma_f32_16x16x32_bf16 v[196:199], v[56:59], v[116:119], v[196:199]\n\t"
      "v_mfma_f32_16x16x32_bf16 v[212:215], v[52:55], v[116:119], v[212:215]\n\t"
      "v_mfma_f32_16x16x32_bf16 v[212:215], v[56:59], v[120:123], v[212:215]\n\t"
      "v_mfma_f32_16x16x32_bf16 v[228:231], v[52:55], v[120:123], v[228:231]\n\t"
      "s_cmp_eq_u32 s34, s35\n\t"
      "v_mfma_f32_16x16x32_bf16 v[228:231], v[56:59], v[124:127], v[228:231]\n\t"
      "s_cbranch_scc1 3b\n\t"
      "4:\n\t"
      "v_mfma_f32_16x16x32_bf16 v[244:247], v[52:55], v[124:127], v[244:247]\n\t"
      "v_mfma_f32_16x16x32_bf16 v[244:247], v[56:59], v[60:63], v[244:247]\n\t"
      "s_waitcnt lgkmcnt(0)\n\t"
      "v_mfma_f32_16x16x32_bf16 v[136:139], v[44:47], v[96:99], v[136:139]\n\t"
      "ds_read_b64_tr_b16 v[52:53], v43 offset:8\n\t"
      "v_mfma_f32_16x16x32_bf16 v[136:139], v[48:51], v[100:103], v[136:139]\n\t"
      "ds_read_b64_tr_b16 v[54:55], v43 offset:1032\n\t"
      "v_mfma_f32_16x16x32_bf16 v[152:155], v[44:47], v[100:103], v[152:155]\n\t"
      "ds_read_b64_tr_b16 v[56:57], v43 offset:8200\n\t"
      "v_mfma_f32_16x16x32_bf16 v[152:155], v[48:51], v[104:107], v[152:155]\n\t"
      "ds_read_b64_tr_b16 v[58:59], v43 offset:9224\n\t"
      "v_mfma_f32_16x16x32_bf16 v[168:171], v[44:47], v[104:107], v[168:171]\n\t"
      "v_mfma_f32_16x16x32_bf16 v[168:171], v[48:51], v[108:111], v[168:171]\n\t"
      "s_add_u32 s36, s36, 0x4000\n\t"
      "v_mfma_f32_16x16x32_bf16 v[184:187], v[44:47], v[108:111], v[184:187]\n\t"
      "s_and_b32 s36, s36, 0xffff\n\t"
      "v_mfma_f32_16x16x32_bf16 v[184:187], v[48:51], v[112:115], v[184:187]\n\t"
      "v_add_u32 v43, s36, v4\n\t"
      "v_mfma_f32_16x16x32_bf16 v[200:203], v[44:47], v[112:115], v[200:203]\n\t"
      "s_add_u32 s33, s33, 0x8000\n\t"
      "v_mfma_f32_16x16x32_bf16 v[200:203], v[48:51], v[116:119], v[200:203]\n\t"
      "s_cmp_eq_u32 s33, 0x18000\n\t"
      "v_mfma_f32_16x16x32_bf16 v[216:219], v[44:47], v[116:119], v[216:219]\n\t"
      "s_cselect_b32 s33, 0, s33\n\t"
      "v_mfma_f32_16x16x32_bf16 v[216:219], v[48:51], v[120:123], v[216:219]\n\t"
      "v_add_u32 v42, s33, v8\n\t"
      "v_mfma_f32_16x16x32_bf16 v[232:235], v[44:47], v[120:123], v[232:235]\n\t"
      "v_add_u32 v41, s33, v9\n\t"
      "v_mfma_f32_16x16x32_bf16 v[232:235], v[48:51], v[124:127], v[232:235]\n\t"
      "v_mfma_f32_16x16x32_bf16 v[248:251], v[44:47], v[124:127], v[248:251]\n\t"
      "s_waitcnt lgkmcnt(0)\n\t"
      "v_mfma_f32_16x16x32_bf16 v[248:251], v[48:51], v[60:63], v[248:251]\n\t"
      "s_barrier\n\t"
      "conv_seg3:\n\t"
      "v_mfma_f32_16x16x32_bf16 v[140:143], v[52:55], v[96:99], v[140:143]\n\t"
      "ds_read_b64_tr_b16 v[44:45], v43 offset:0\n\t"
      "v_mfma_f32_16x16x32_bf16 v[140:143], v[56:59], v[100:103], v[140:143]\n\t"
      "ds_read_b64_tr_b16 v[46:47], v43 offset:1024\n\t"
      "v_mfma_f32_16x16x32_bf16 v[156:159], v[52:55], v[100:103], v[156:159]\n\t"
      "ds_read_b128 v[64:67], v42 offset:0\n\t"
      "v_mfma_f32_16x16x32_bf16 v[156:159], v[56:59], v[104:107], v[156:159]\n\t"
      "ds_read_b64_tr_b16 v[48:49], v43 offset:8192\n\t"
      "v_mfma_f32_16x16x32_bf16 v[172:175], v[52:55], v[104:107], v[172:175]\n\t"
      "ds_read_b64_tr_b16 v[50:51], v43 offset:9216\n\t"
      "v_mfma_f32_16x16x32_bf16 v[172:175], v[56:59], v[108:111], v[172:175]\n\t"
      "ds_read_b128 v[68:71], v41 offset:0\n\t"
      "v_mfma_f32_16x16x32_bf16 v[188:191], v[52:55], v[108:111], v[188:191]\n\t"
      "ds_read_b128 v[72:75], v41 offset:4096\n\t"
      "v_mfma_f32_16x16x32_bf16 v[188:191], v[56:59], v[112:115], v[188:191]\n\t"
      "ds_read_b128 v[76:79], v42 offset:4096\n\t"
      "v_mfma_f32_16x16x32_bf16 v[204:207], v[52:55], v[112:115], v[204:207]\n\t"
      "ds_read_b128 v[80:83], v42 offset:8192\n\t"
      "v_mfma_f32_16x16x32_bf16 v[204:207], v[56:59], v[116:119], v[204:207]\n\t"
      "ds_read_b128 v[84:87], v41 offset:8192\n\t"
      "v_mfma_f32_16x16x32_bf16 v[220:223], v[52:55], v[116:119], v[220:223]\n\t"
      "ds_read_b128 v[88:91], v41 offset:12288\n\t"
      "v_mfma_f32_16x16x32_bf16 v[220:223], v[56:59], v[120:123], v[220:223]\n\t"
      "ds_read_b128 v[92:95], v42 offset:12288\n\t"
      "v_mfma_f32_16x16x32_bf16 v[236:239], v[52:55], v[120:123], v[236:239]\n\t"
      "ds_read_b128 v[96:99], v42 offset:16384\n\t"
      "v_mfma_f32_16x16x32_bf16 v[236:239], v[56:59], v[124:127], v[236:239]\n\t"
      "ds_read_b128 v[100:103], v41 offset:16384\n\t"
      "v_mfma_f32_16x16x32_bf16 v[252:255], v[52:55], v[124:127], v[252:255]\n\t"
      "ds_read_b128 v[104:107], v41 offset:20480\n\t"
      "v_mfma_f32_16x16x32_bf16 v[252:255], v[56:59], v[60:63], v[252:255]\n\t"
      "s_waitcnt lgkmcnt(7)\n\t"
      "ds_read_b128 v[108:111], v42 offset:20480\n\t"
      "conv_o0:\n\t"
      "v_mfma_f32_16x16x32_bf16 v[128:131], v[44:47], v[64:67], v[128:131]\n\t"
      "ds_read_b128 v[112:115], v42 offset:24576\n\t"
      "v_mfma_f32_16x16x32_bf16 v[128:131], v[48:51], v[68:71], v[128:131]\n\t"
      "ds_read_b128 v[116:119], v41 offset:24576\n\t"
      "v_mfma_f32_16x16x32_bf16 v[144:147], v[48:51], v[72:75], v[144:147]\n\t"
      "ds_read_b128 v[120:123], v41 offset:28672\n\t"
      "v_mfma_f32_16x16x32_bf16 v[144:147], v[44:47], v[76:79], v[144:147]\n\t"
      "ds_read_b128 v[124:127], v42 offset:28672\n\t"
      "s_waitcnt lgkmcnt(10)\n\t"
      "v_mfma_f32_16x16x32_bf16 v[160:163], v[44:47], v[80:83], v[160:163]\n\t"
      "ds_read_b64_tr_b16 v[52:53], v43 offset:8\n\t"
      "v_mfma_f32_16x16x32_bf16 v[160:163], v[48:51], v[84:87], v[160:163]\n\t"
      "ds_read_b64_tr_b16 v[54:55], v43 offset:1032\n\t"
      "s_waitcnt lgkmcnt(10)\n\t"
      "v_mfma_f32_16x16x32_bf16 v[176:179], v[48:51], v[88:91], v[176:179]\n\t"
      "ds_read_b64_tr_b16 v[56:57], v43 offset:8200\n\t"
      "v_mfma_f32_16x16x32_bf16 v[176:179], v[44:47], v[92:95], v[176:179]\n\t"
      "ds_read_b64_tr_b16 v[58:59], v43 offset:9224\n\t"
      "s_waitcnt lgkmcnt(10)\n\t"
      "v_mfma_f32_16x16x32_bf16 v[192:195], v[44:47], v[96:99], v[192:195]\n\t"
      "v_mfma_f32_16x16x32_bf16 v[192:195], v[48:51], v[100:103], v[192:195]\n\t"
      "v_add_u32 v43, s36, v5\n\t"
      "s_waitcnt lgkmcnt(8)\n\t"
      "v_mfma_f32_16x16x32_bf16 v[208:211], v[48:51], v[104:107], v[208:211]\n\t"
      "v_mfma_f32_16x16x32_bf16 v[208:211], v[44:47], v[108:111], v[208:211]\n\t"
      "s_waitcnt lgkmcnt(6)\n\t"
      "v_mfma_f32_16x16x32_bf16 v[224:227], v[44:47], v[112:115], v[224:227]\n\t"
      "v_mfma_f32_16x16x32_bf16 v[224:227], v[48:51], v[116:119], v[224:227]\n\t"
      "s_waitcnt lgkmcnt(4)\n\t"
      "v_mfma_f32_16x16x32_bf16 v[240:243], v[48:51], v[120:123], v[240:243]\n\t"
      "v_mfma_f32_16x16x32_bf16 v[240:243], v[44:47], v[124:127], v[240:243]\n\t"
      "s_waitcnt lgkmcnt(0)\n\t"
      "v_mfma_f32_16x16x32_bf16 v[132:135], v[52:55], v[64:67], v[132:135]\n\t"
      "ds_read_b64_tr_b16 v[44:45], v43 offset:0\n\t"
      "v_mfma_f32_16x16x32_bf16 v[132:135], v[56:59], v[68:71], v[132:135]\n\t"
      "ds_read_b64_tr_b16 v[46:47], v43 offset:1024\n\t"
      "v_mfma_f32_16x16x32_bf16 v[148:151], v[56:59], v[72:75], v[148:151]\n\t"
      "ds_read_b64_tr_b16 v[48:49], v43 offset:8192\n\t"
      "v_mfma_f32_16x16x32_bf16 v[148:151], v[52:55], v[76:79], v[148:151]\n\t"
      "ds_read_b64_tr_b16 v[50:51], v43 offset:9216\n\t"
      "v_mfma_f32_16x16x32_bf16 v[164:167], v[52:55], v[80:83], v[164:167]\n\t"
      "v_mfma_f32_16x16x32_bf16 v[164:167], v[56:59], v[84:87], v[164:167]\n\t"
      "v_mfma_f32_16x16x32_bf16 v[180:183], v[56:59], v[88:91], v[180:183]\n\t"
      "v_mfma_f32_16x16x32_bf16 v[180:183], v[52:55], v[92:95], v[180:183]\n\t"
      "v_mfma_f32_16x16x32_bf16 v[196:199], v[52:55], v[96:99], v[196:199]\n\t"
      "v_mfma_f32_16x16x32_bf16 v[196:199], v[56:59], v[100:103], v[196:199]\n\t"
      "v_mfma_f32_16x16x32_bf16 v[212:215], v[56:59], v[104:107], v[212:215]\n\t"
      "v_mfma_f32_16x16x32_bf16 v[212:215], v[52:55], v[108:111], v[212:215]\n\t"
      "v_mfma_f32_16x16x32_bf16 v[228:231], v[52:55], v[112:115], v[228:231]\n\t"
      "v_mfma_f32_16x16x32_bf16 v[228:231], v[56:59], v[116:119], v[228:231]\n\t"
      "v_mfma_f32_16x16x32_bf16 v[244:247], v[56:59], v[120:123], v[244:247]\n\t"
      "v_mfma_f32_16x16x32_bf16 v[244:247], v[52:55], v[124:127], v[244:247]\n\t"
      "s_waitcnt lgkmcnt(0)\n\t"
      "v_mfma_f32_16x16x32_bf16 v[136:139], v[44:47], v[64:67], v[136:139]\n\t"
      "ds_read_b64_tr_b16 v[52:53], v43 offset:8\n\t"
      "v_mfma_f32_16x16x32_bf16 v[136:139], v[48:51], v[68:71], v[136:139]\n\t"
      "ds_read_b64_tr_b16 v[54:55], v43 offset:1032\n\t"
      "v_mfma_f32_16x16x32_bf16 v[152:155], v[48:51], v[72:75], v[152:155]\n\t"
      "ds_read_b64_tr_b16 v[56:57], v43 offset:8200\n\t"
      "v_mfma_f32_16x16x32_bf16 v[152:155], v[44:47], v[76:79], v[152:155]\n\t"
      "ds_read_b64_tr_b16 v[58:59], v43 offset:9224\n\t"
      "v_mfma_f32_16x16x32_bf16 v[168:171], v[44:47], v[80:83], v[168:171]\n\t"
      "v_mfma_f32_16x16x32_bf16 v[168:171], v[48:51], v[84:87], v[168:171]\n\t"
      "s_add_u32 s36, s36, 0x4000\n\t"
      "v_mfma_f32_16x16x32_bf16 v[184:187], v[48:51], v[88:91], v[184:187]\n\t"
      "s_and_b32 s36, s36, 0xffff\n\t"
      "v_mfma_f32_16x16x32_bf16 v[184:187], v[44:47], v[92:95], v[184:187]\n\t"
      "v_add_u32 v43, s36, v4\n\t"
      "v_mfma_f32_16x16x32_bf16 v[200:203], v[44:47], v[96:99], v[200:203]\n\t"
      "s_add_u32 s33, s33, 0x8000\n\t"
      "v_mfma_f32_16x16x32_bf16 v[200:203], v[48:51], v[100:103], v[200:203]\n\t"
      "s_cmp_eq_u32 s33, 0x18000\n\t"
      "v_mfma_f32_16x16x32_bf16 v[216:219], v[48:51], v[104:107], v[216:219]\n\t"
      "s_cselect_b32 s33, 0, s33\n\t"
      "v_mfma_f32_16x16x32_bf16 v[216:219], v[44:47], v[108:111], v[216:219]\n\t"
      "v_add_u32 v42, s33, v8\n\t"
      "v_mfma_f32_16x16x32_bf16 v[232:235], v[44:47], v[112:115], v[232:235]\n\t"
      "v_add_u32 v41, s33, v10\n\t"
      "v_mfma_f32_16x16x32_bf16 v[232:235], v[48:51], v[116:119], v[232:235]\n\t"
      "s_sub_u32 s34, s34, 1\n\t"
      "v_mfma_f32_16x16x32_bf16 v[248:251], v[48:51], v[120:123], v[248:251]\n\t"
      "s_cmp_lg_u32 s34, 0\n\t"
      "s_waitcnt lgkmcnt(0)\n\t"
      "v_mfma_f32_16x16x32_bf16 v[248:251], v[44:47], v[124:127], v[248:251]\n\t"
      "s_cbranch_scc1 conv_loop\n\t"
      "conv_tail:\n\t"
      "s_barrier\n\t"
      "s_cmp_eq_u32 s3, 7\n\t"
      "s_cbranch_scc0 conv_noname\n\t"
      "ds_write_b64 v40, v[38:39] offset:39168\n\t"
      "conv_noname:\n\t"
      "v_mfma_f32_16x16x32_bf16 v[140:143], v[52:55], v[64:67], v[140:143]\n\t"
      "ds_read_b64_tr_b16 v[44:45], v43 offset:0\n\t"
      "v_mfma_f32_16x16x32_bf16 v[140:143], v[56:59], v[68:71], v[140:143]\n\t"
      "ds_read_b64_tr_b16 v[46:47], v43 offset:1024\n\t"
      "v_mfma_f32_16x16x32_bf16 v[156:159], v[56:59], v[72:75], v[156:159]\n\t"
      "ds_read_b128 v[60:63], v42 offset:0\n\t"
      "v_mfma_f32_16x16x32_bf16 v[156:159], v[52:55], v[76:79], v[156:159]\n\t"
      "ds_read_b64_tr_b16 v[48:49], v43 offset:8192\n\t"
      "v_mfma_f32_16x16x32_bf16 v[172:175], v[52:55], v[80:83], v[172:175]\n\t"
      "ds_read_b64_tr_b16 v[50:51], v43 offset:9216\n\t"
      "v_mfma_f32_16x16x32_bf16 v[172:175], v[56:59], v[84:87], v[172:175]\n\t"
      "ds_read_b128 v[64:67], v42 offset:4096\n\t"
      "v_mfma_f32_16x16x32_bf16 v[188:191], v[56:59], v[88:91], v[188:191]\n\t"
      "ds_read_b128 v[68:71], v42 offset:8192\n\t"
      "v_mfma_f32_16x16x32_bf16 v[188:191], v[52:55], v[92:95], v[188:191]\n\t"
      "ds_read_b128 v[72:75], v42 offset:12288\n\t"
      "v_mfma_f32_16x16x32_bf16 v[204:207], v[52:55], v[96:99], v[204:207]\n\t"
      "ds_read_b128 v[76:79], v42 offset:16384\n\t"
      "v_mfma_f32_16x16x32_bf16 v[204:207], v[56:59], v[100:103], v[204:207]\n\t"
      "ds_read_b128 v[80:83], v42 offset:20480\n\t"
      "v_mfma_f32_16x16x32_bf16 v[220:223], v[56:59], v[104:107], v[220:223]\n\t"
      "ds_read_b128 v[84:87], v42 offset:24576\n\t"
      "v_mfma_f32_16x16x32_bf16 v[220:223], v[52:55], v[108:111], v[220:223]\n\t"
      "ds_read_b128 v[88:91], v42 offset:28672\n\t"
      "v_mfma_f32_16x16x32_bf16 v[236:239], v[52:55], v[112:115], v[236:239]\n\t"
      "ds_read_b128 v[92:95], v41\n\t"
      "v_mfma_f32_16x16x32_bf16 v[236:239], v[56:59], v[116:119], v[236:239]\n\t"
      "v_mfma_f32_16x16x32_bf16 v[252:255], v[56:59], v[120:123], v[252:255]\n\t"
      "v_mfma_f32_16x16x32_bf16 v[252:255], v[52:55], v[124:127], v[252:255]\n\t"
      "s_waitcnt lgkmcnt(7)\n\t"
      "v_mfma_f32_16x16x32_bf16 v[128:131], v[44:47], v[60:63], v[128:131]\n\t"
      "ds_read_b64_tr_b16 v[52:53], v43 offset:8\n\t"
      "v_mfma_f32_16x16x32_bf16 v[128:131], v[48:51], v[64:67], v[128:131]\n\t"
      "ds_read_b64_tr_b16 v[54:55], v43 offset:1032\n\t"
      "s_waitcnt lgkmcnt(8)\n\t"
      "v_mfma_f32_16x16x32_bf16 v[144:147], v[44:47], v[64:67], v[144:147]\n\t"
      "ds_read_b64_tr_b16 v[56:57], v43 offset:8200\n\t"
      "v_mfma_f32_16x16x32_bf16 v[144:147], v[48:51], v[68:71], v[144:147]\n\t"
      "ds_read_b64_tr_b16 v[58:59], v43 offset:9224\n\t"
      "s_waitcnt lgkmcnt(9)\n\t"
      "v_mfma_f32_16x16x32_bf16 v[160:163], v[44:47], v[68:71], v[160:163]\n\t"
      "v_mfma_f32_16x16x32_bf16 v[160:163], v[48:51], v[72:75], v[160:163]\n\t"
      "s_waitcnt lgkmcnt(8)\n\t"
      "v_mfma_f32_16x16x32_bf16 v[176:179], v[44:47], v[72:75], v[176:179]\n\t"
      "v_mfma_f32_16x16x32_bf16 v[176:179], v[48:51], v[76:79], v[176:179]\n\t"
      "s_waitcnt lgkmcnt(7)\n\t"
      "v_mfma_f32_16x16x32_bf16 v[192:195], v[44:47], v[76:79], v[192:195]\n\t"
      "v_mfma_f32_16x16x32_bf16 v[192:195], v[48:51], v[80:83], v[192:195]\n\t"
      "s_waitcnt lgkmcnt(6)\n\t"
      "v_mfma_f32_16x16x32_bf16 v[208:211], v[44:47], v[80:83], v[208:211]\n\t"
      "v_add_u32 v43, 32768, v4\n\t"
      "v_mfma_f32_16x16x32_bf16 v[208:211], v[48:51], v[84:87], v[208:211]\n\t"
      "v_add_u32 v42, 65536, v9\n\t"
      "s_waitcnt lgkmcnt(5)\n\t"
      "v_mfma_f32_16x16x32_bf16 v[224:227], v[44:47], v[84:87], v[224:227]\n\t"
      "v_add_u32 v41, 65536, v11\n\t"
      "v_mfma_f32_16x16x32_bf16 v[224:227], v[48:51], v[88:91], v[224:227]\n\t"
      "s_waitcnt lgkmcnt(4)\n\t"
      "v_mfma_f32_16x16x32_bf16 v[240:243], v[44:47], v[88:91], v[240:243]\n\t"
      "s_waitcnt lgkmcnt(0)\n\t"
      "v_mfma_f32_16x16x32_bf16 v[240:243], v[48:51], v[92:95], v[240:243]\n\t"
      "s_barrier\n\t"
      "v_mfma_f32_16x16x32_bf16 v[132:135], v[52:55], v[60:63], v[132:135]\n\t"
      "ds_read_b64_tr_b16 v[44:45], v43 offset:0\n\t"
      "v_mfma_f32_16x16x32_bf16 v[132:135], v[56:59], v[64:67], v[132:135]\n\t"
      "ds_read_b64_tr_b16 v[46:47], v43 offset:1024\n\t"
      "v_mfma_f32_16x16x32_bf16 v[148:151], v[52:55], v[64:67], v[148:151]\n\t"
      "ds_read_b128 v[96:99], v42 offset:0\n\t"
      "v_mfma_f32_16x16x32_bf16 v[148:151], v[56:59], v[68:71], v[148:151]\n\t"
      "ds_read_b64_tr_b16 v[48:49], v43 offset:8192\n\t"
      "v_mfma_f32_16x16x32_bf16 v[164:167], v[52:55], v[68:71], v[164:167]\n\t"
      "ds_read_b64_tr_b16 v[50:51], v43 offset:9216\n\t"
      "v_mfma_f32_16x16x32_bf16 v[164:167], v[56:59], v[72:75], v[164:167]\n\t"
      "ds_read_b128 v[100:103], v42 offset:4096\n\t"
      "v_mfma_f32_16x16x32_bf16 v[180:183], v[52:55], v[72:75], v[180:183]\n\t"
      "ds_read_b128 v[104:107], v42 offset:8192\n\t"
      "v_mfma_f32_16x16x32_bf16 v[180:183], v[56:59], v[76:79], v[180:183]\n\t"
      "ds_read_b128 v[108:111], v42 offset:12288\n\t"
      "v_mfma_f32_16x16x32_bf16 v[196:199], v[52:55], v[76:79], v[196:199]\n\t"
      "ds_read_b128 v[112:115], v42 offset:16384\n\t"
      "v_mfma_f32_16x16x32_bf16 v[196:199], v[56:59], v[80:83], v[196:199]\n\t"
      "ds_read_b128 v[116:119], v42 offset:20480\n\t"
      "v_mfma_f32_16x16x32_bf16 v[212:215], v[52:55], v[80:83], v[212:215]\n\t"
      "ds_read_b128 v[120:123], v42 offset:24576\n\t"
      "v_mfma_f32_16x16x32_bf16 v[212:215], v[56:59], v[84:87], v[212:215]\n\t"
      "ds_read_b128 v[124:127], v42 offset:28672\n\t"
      "v_mfma_f32_16x16x32_bf16 v[228:231], v[52:55], v[84:87], v[228:231]\n\t"
      "ds_read_b128 v[60:63], v41\n\t"
      "v_mfma_f32_16x16x32_bf16 v[228:231], v[56:59], v[88:91], v[228:231]\n\t"
      "v_mfma_f32_16x16x32_bf16 v[244:247], v[52:55], v[88:91], v[244:247]\n\t"
      "v_mfma_f32_16x16x32_bf16 v[244:247], v[56:59], v[92:95], v[244:247]\n\t"
      "s_waitcnt lgkmcnt(7)\n\t"
      "v_mfma_f32_16x16x32_bf16 v[128:131], v[44:47], v[96:99], v[128:131]\n\t"
      "ds_read_b64_tr_b16 v[52:53], v43 offset:8\n\t"
      "v_mfma_f32_16x16x32_bf16 v[128:131], v[48:51], v[100:103], v[128:131]\n\t"
      "ds_read_b64_tr_b16 v[54:55], v43 offset:1032\n\t"
      "s_waitcnt lgkmcnt(8)\n\t"
      "v_mfma_f32_16x16x32_bf16 v[144:147], v[44:47], v[100:103], v[144:147]\n\t"
      "ds_read_b64_tr_b16 v[56:57], v43 offset:8200\n\t"
      "v_mfma_f32_16x16x32_bf16 v[144:147], v[48:51], v[104:107], v[144:147]\n\t"
      "ds_read_b64_tr_b16 v[58:59], v43 offset:9224\n\t"
      "s_waitcnt lgkmcnt(9)\n\t"
      "v_mfma_f32_16x16x32_bf16 v[160:163], v[44:47], v[104:107], v[160:163]\n\t"
      "v_mfma_f32_16x16x32_bf16 v[160:163], v[48:51], v[108:111], v[160:163]\n\t"
      "s_waitcnt lgkmcnt(8)\n\t"
      "v_mfma_f32_16x16x32_bf16 v[176:179], v[44:47], v[108:111], v[176:179]\n\t"
      "v_mfma_f32_16x16x32_bf16 v[176:179], v[48:51], v[112:115], v[176:179]\n\t"
      "s_waitcnt lgkmcnt(7)\n\t"
      "v_mfma_f32_16x16x32_bf16 v[192:195], v[44:47], v[112:115], v[192:195]\n\t"
      "v_mfma_f32_16x16x32_bf16 v[192:195], v[48:51], v[116:119], v[192:195]\n\t"
      "s_waitcnt lgkmcnt(6)\n\t"
      "v_mfma_f32_16x16x32_bf16 v[208:211], v[44:47], v[116:119], v[208:211]\n\t"
      "v_add_u32 v43, 49152, v4\n\t"
      "v_mfma_f32_16x16x32_bf16 v[208:211], v[48:51], v[120:123], v[208:211]\n\t"
      "v_add_u32 v42, 0, v8\n\t"
      "s_waitcnt lgkmcnt(5)\n\t"
      "v_mfma_f32_16x16x32_bf16 v[224:227], v[44:47], v[120:123], v[224:227]\n\t"
      "v_add_u32 v41, 0, v9\n\t"
      "v_mfma_f32_16x16x32_bf16 v[224:227], v[48:51], v[124:127], v[224:227]\n\t"
      "s_waitcnt lgkmcnt(4)\n\t"
      "v_mfma_f32_16x16x32_bf16 v[240:243], v[44:47], v[124:127], v[240:243]\n\t"
      "v_mfma_f32_16x16x32_bf16 v[240:243], v[48:51], v[60:63], v[240:243]\n\t"
      "s_barrier\n\t"
      "s_waitcnt lgkmcnt(0)\n\t"
      "v_mfma_f32_16x16x32_bf16 v[132:135], v[52:55], v[96:99], v[132:135]\n\t"
      "ds_read_b64_tr_b16 v[44:45], v43 offset:0\n\t"
      "v_mfma_f32_16x16x32_bf16 v[132:135], v[56:59], v[100:103], v[132:135]\n\t"
      "ds_read_b64_tr_b16 v[46:47], v43 offset:1024\n\t"
      "v_mfma_f32_16x16x32_bf16 v[148:151], v[52:55], v[100:103], v[148:151]\n\t"
      "ds_read_b128 v[64:67], v42 offset:0\n\t"
      "v_mfma_f32_16x16x32_bf16 v[148:151], v[56:59], v[104:107], v[148:151]\n\t"
      "ds_read_b64_tr_b16 v[48:49], v43 offset:8192\n\t"
      "v_mfma_f32_16x16x32_bf16 v[164:167], v[52:55], v[104:107], v[164:167]\n\t"
      "ds_read_b64_tr_b16 v[50:51], v43 offset:9216\n\t"
      "v_mfma_f32_16x16x32_bf16 v[164:167], v[56:59], v[108:111], v[164:167]\n\t"
      "ds_read_b128 v[68:71], v41 offset:0\n\t"
      "v_mfma_f32_16x16x32_bf16 v[180:183], v[52:55], v[108:111], v[180:183]\n\t"
      "ds_read_b128 v[72:75], v41 offset:4096\n\t"
      "v_mfma_f32_16x16x32_bf16 v[180:183], v[56:59], v[112:115], v[180:183]\n\t"
      "ds_read_b128 v[76:79], v42 offset:4096\n\t"
      "v_mfma_f32_16x16x32_bf16 v[196:199], v[52:55], v[112:115], v[196:199]\n\t"
      "ds_read_b128 v[80:83], v42 offset:8192\n\t"
      "v_mfma_f32_16x16x32_bf16 v[196:199], v[56:59], v[116:119], v[196:199]\n\t"
      "ds_read_b128 v[84:87], v41 offset:8192\n\t"
      "v_mfma_f32_16x16x32_bf16 v[212:215], v[52:55], v[116:119], v[212:215]\n\t"
      "ds_read_b128 v[88:91], v41 offset:12288\n\t"
      "v_mfma_f32_16x16x32_bf16 v[212:215], v[56:59], v[120:123], v[212:215]\n\t"
      "ds_read_b128 v[92:95], v42 offset:12288\n\t"
      "v_mfma_f32_16x16x32_bf16 v[228:231], v[52:55], v[120:123], v[228:231]\n\t"
      "ds_read_b128 v[96:99], v42 offset:16384\n\t"
      "v_mfma_f32_16x16x32_bf16 v[228:231], v[56:59], v[124:127], v[228:231]\n\t"
      "ds_read_b128 v[100:103], v41 offset:16384\n\t"
      "v_mfma_f32_16x16x32_bf16 v[244:247], v[52:55], v[124:127], v[244:247]\n\t"
      "ds_read_b128 v[104:107], v41 offset:20480\n\t"
      "v_mfma_f32_16x16x32_bf16 v[244:247], v[56:59], v[60:63], v[244:247]\n\t"
      "s_waitcnt lgkmcnt(7)\n\t"
      "ds_read_b128 v[108:111], v42 offset:20480\n\t"
      "v_mfma_f32_16x16x32_bf16 v[128:131], v[44:47], v[64:67], v[128:131]\n\t"
      "ds_read_b128 v[112:115], v42 offset:24576\n\t"
      "v_mfma_f32_16x16x32_bf16 v[128:131], v[48:51], v[68:71], v[128:131]\n\t"
      "ds_read_b128 v[116:119], v41 offset:24576\n\t"
      "v_mfma_f32_16x16x32_bf16 v[144:147], v[48:51], v[72:75], v[144:147]\n\t"
      "ds_read_b128 v[120:123], v41 offset:28672\n\t"
      "v_mfma_f32_16x16x32_bf16 v[144:147], v[44:47], v[76:79], v[144:147]\n\t"
      "ds_read_b128 v[124:127], v42 offset:28672\n\t"
      "s_waitcnt lgkmcnt(10)\n\t"
      "v_mfma_f32_16x16x32_bf16 v[160:163], v[44:47], v[80:83], v[160:163]\n\t"
      "ds_read_b64_tr_b16 v[52:53], v43 offset:8\n\t"
      "v_mfma_f32_16x16x32_bf16 v[160:163], v[48:51], v[84:87], v[160:163]\n\t"
      "ds_read_b64_tr_b16 v[54:55], v43 offset:1032\n\t"
      "s_waitcnt lgkmcnt(10)\n\t"
      "v_mfma_f32_16x16x32_bf16 v[176:179], v[48:51], v[88:91], v[176:179]\n\t"
      "ds_read_b64_tr_b16 v[56:57], v43 offset:8200\n\t"
      "v_mfma_f32_16x16x32_bf16 v[176:179], v[44:47], v[92:95], v[176:179]\n\t"
      "ds_read_b64_tr_b16 v[58:59], v43 offset:9224\n\t"
      "s_waitcnt lgkmcnt(10)\n\t"
      "v_mfma_f32_16x16x32_bf16 v[192:195], v[44:47], v[96:99], v[192:195]\n\t"
      "v_mfma_f32_16x16x32_bf16 v[192:195], v[48:51], v[100:103], v[192:195]\n\t"
      "s_waitcnt lgkmcnt(8)\n\t"
      "v_mfma_f32_16x16x32_bf16 v[208:211], v[48:51], v[104:107], v[208:211]\n\t"
      "v_add_u32 v43, 16384, v5\n\t"
      "v_mfma_f32_16x16x32_bf16 v[208:211], v[44:47], v[108:111], v[208:211]\n\t"
      "v_add_u32 v42, 65536, v8\n\t"
      "s_waitcnt lgkmcnt(6)\n\t"
      "v_mfma_f32_16x16x32_bf16 v[224:227], v[44:47], v[112:115], v[224:227]\n\t"
      "v_add_u32 v41, 65536, v10\n\t"
      "v_mfma_f32_16x16x32_bf16 v[224:227], v[48:51], v[116:119], v[224:227]\n\t"
      "s_waitcnt lgkmcnt(4)\n\t"
      "v_mfma_f32_16x16x32_bf16 v[240:243], v[48:51], v[120:123], v[240:243]\n\t"
      "v_mfma_f32_16x16x32_bf16 v[240:243], v[44:47], v[124:127], v[240:243]\n\t"
      "s_waitcnt lgkmcnt(0)\n\t"
      "v_mfma_f32_16x16x32_bf16 v[132:135], v[52:55], v[64:67], v[132:135]\n\t"
      "ds_read_b64_tr_b16 v[44:45], v43 offset:0\n\t"
      "v_mfma_f32_16x16x32_bf16 v[132:135], v[56:59], v[68:71], v[132:135]\n\t"
      "ds_read_b64_tr_b16 v[46:47], v43 offset:1024\n\t"
      "v_mfma_f32_16x16x32_bf16 v[148:151], v[56:59], v[72:75], v[148:151]\n\t"
      "ds_read_b128 v[60:63], v42 offset:0\n\t"
      "v_mfma_f32_16x16x32_bf16 v[148:151], v[52:55], v[76:79], v[148:151]\n\t"
      "ds_read_b64_tr_b16 v[48:49], v43 offset:8192\n\t"
      "v_mfma_f32_16x16x32_bf16 v[164:167], v[52:55], v[80:83], v[164:167]\n\t"
      "ds_read_b64_tr_b16 v[50:51], v43 offset:9216\n\t"
      "v_mfma_f32_16x16x32_bf16 v[164:167], v[56:59], v[84:87], v[164:167]\n\t"
      "ds_read_b128 v[64:67], v42 offset:4096\n\t"
      "v_mov_b32_dpp v18, v128 row_newbcast:0 row_mask:0xf bank_mask:0xf\n\t"
      "v_mfma_f32_16x16x32_bf16 v[180:183], v[56:59], v[88:91], v[180:183]\n\t"
      "ds_read_b128 v[68:71], v42 offset:8192\n\t"
      "v_mov_b32_dpp v19, v129 row_newbcast:0 row_mask:0xf bank_mask:0xf\n\t"
      "v_mfma_f32_16x16x32_bf16 v[180:183], v[52:55], v[92:95], v[180:183]\n\t"
      "ds_read_b128 v[72:75], v42 offset:12288\n\t"
      "v_mov_b32_dpp v20, v130 row_newbcast:0 row_mask:0xf bank_mask:0xf\n\t"
      "v_mfma_f32_16x16x32_bf16 v[196:199], v[52:55], v[96:99], v[196:199]\n\t"
      "ds_read_b128 v[76:79], v42 offset:16384\n\t"
      "v_mov_b32_dpp v21, v131 row_newbcast:0 row_mask:0xf bank_mask:0xf\n\t"
      "v_mfma_f32_16x16x32_bf16 v[196:199], v[56:59], v[100:103], v[196:199]\n\t"
      "ds_read_b128 v[80:83], v42 offset:20480\n\t"
      "v_mfma_f32_16x16x32_bf16 v[212:215], v[56:59], v[104:107], v[212:215]\n\t"
      "ds_read_b128 v[84:87], v42 offset:24576\n\t"
      "v_mov_b32_dpp v22, v132 row_newbcast:0 row_mask:0xf bank_mask:0xf\n\t"
      "v_mfma_f32_16x16x32_bf16 v[212:215], v[52:55], v[108:111], v[212:215]\n\t"
      "ds_read_b128 v[88:91], v42 offset:28672\n\t"
      "v_mov_b32_dpp v23, v133 row_newbcast:0 row_mask:0xf bank_mask:0xf\n\t"
      "v_mfma_f32_16x16x32_bf16 v[228:231], v[52:55], v[112:115], v[228:231]\n\t"
      "ds_read_b128 v[92:95], v41\n\t"
      "v_mov_b32_dpp v24, v134 row_newbcast:0 row_mask:0xf bank_mask:0xf\n\t"
      "v_mfma_f32_16x16x32_bf16 v[228:231], v[56:59], v[116:119], v[228:231]\n\t"
      "v_mov_b32_dpp v25, v135 row_newbcast:0 row_mask:0xf bank_mask:0xf\n\t"
      "v_mfma_f32_16x16x32_bf16 v[244:247], v[56:59], v[120:123], v[244:247]\n\t"
      "v_mfma_f32_16x16x32_bf16 v[244:247], v[52:55], v[124:127], v[244:247]\n\t"
      "s_waitcnt lgkmcnt(0)\n\t"
      "v_pk_add_f32 v[96:97], v[128:129], v[18:19] neg_lo:[0,1] neg_hi:[0,1]\n\t"
      "v_pk_add_f32 v[98:99], v[130:131], v[20:21] neg_lo:[0,1] neg_hi:[0,1]\n\t"
      "v_pk_add_f32 v[100:101], v[132:133], v[22:23] neg_lo:[0,1] neg_hi:[0,1]\n\t"
      "v_pk_add_f32 v[102:103], v[134:135], v[24:25] neg_lo:[0,1] neg_hi:[0,1]\n\t"
      "v_pk_add_f32 v[128:129], v[128:129], v[18:19] neg_lo:[0,1] neg_hi:[0,1]\n\t"
      "v_pk_add_f32 v[130:131], v[130:131], v[20:21] neg_lo:[0,1] neg_hi:[0,1]\n\t"
      "v_pk_add_f32 v[132:133], v[132:133], v[22:23] neg_lo:[0,1] neg_hi:[0,1]\n\t"
      "v_pk_add_f32 v[134:135], v[134:135], v[24:25] neg_lo:[0,1] neg_hi:[0,1]\n\t"
      "v_pk_mul_f32 v[104:105], v[96:97], v[96:97]\n\t"
      "v_pk_mul_f32 v[106:107], v[98:99], v[98:99]\n\t"
      "v_pk_mul_f32 v[108:109], v[100:101], v[100:101]\n\t"
      "v_pk_mul_f32 v[110:111], v[102:103], v[102:103]\n\t"
      "s_mov_b64 exec, s[56:57]\n\t"
      "v_pk_add_f32 v[144:145], v[144:145], v[18:19] neg_lo:[0,1] neg_hi:[0,1]\n\t"
      "v_pk_add_f32 v[146:147], v[146:147], v[20:21] neg_lo:[0,1] neg_hi:[0,1]\n\t"
      "v_pk_add_f32 v[148:149], v[148:149], v[22:23] neg_lo:[0,1] neg_hi:[0,1]\n\t"
      "v_pk_add_f32 v[150:151], v[150:151], v[24:25] neg_lo:[0,1] neg_hi:[0,1]\n\t"
      "v_pk_add_f32 v[96:97], v[96:97], v[144:145]\n\t"
      "v_pk_add_f32 v[98:99], v[98:99], v[146:147]\n\t"
      "v_pk_add_f32 v[100:101], v[100:101], v[148:149]\n\t"
      "v_pk_add_f32 v[102:103], v[102:103], v[150:151]\n\t"
      "v_pk_fma_f32 v[104:105], v[144:145], v[144:145], v[104:105]\n\t"
      "v_pk_fma_f32 v[106:107], v[146:147], v[146:147], v[106:107]\n\t"
      "v_pk_fma_f32 v[108:109], v[148:149], v[148:149], v[108:109]\n\t"
      "v_pk_fma_f32 v[110:111], v[150:151], v[150:151], v[110:111]\n\t"
      "s_mov_b64 exec, s[58:59]\n\t"
      "v_pk_add_f32 v[160:161], v[160:161], v[18:19] neg_lo:[0,1] neg_hi:[0,1]\n\t"
      "v_pk_add_f32 v[162:163], v[162:163], v[20:21] neg_lo:[0,1] neg_hi:[0,1]\n\t"
      "v_pk_add_f32 v[164:165], v[164:165], v[22:23] neg_lo:[0,1] neg_hi:[0,1]\n\t"
      "v_pk_add_f32 v[166:167], v[166:167], v[24:25] neg_lo:[0,1] neg_hi:[0,1]\n\t"
      "v_pk_add_f32 v[96:97], v[96:97], v[160:161]\n\t"
      "v_pk_add_f32 v[98:99], v[98:99], v[162:163]\n\t"
      "v_pk_add_f32 v[100:101], v[100:101], v[164:165]\n\t"
      "v_pk_add_f32 v[102:103], v[102:103], v[166:167]\n\t"
      "v_pk_fma_f32 v[104:105], v[160:161], v[160:161], v[104:105]\n\t"
      "v_pk_fma_f32 v[106:107], v[162:163], v[162:163], v[106:107]\n\t"
      "v_pk_fma_f32 v[108:109], v[164:165], v[164:165], v[108:109]\n\t"
      "v_pk_fma_f32 v[110:111], v[166:167], v[166:167], v[110:111]\n\t"
      "v_pk_add_f32 v[176:177], v[176:177], v[18:19] neg_lo:[0,1] neg_hi:[0,1]\n\t"
      "v_pk_add_f32 v[178:179], v[178:179], v[20:21] neg_lo:[0,1] neg_hi:[0,1]\n\t"
      "v_pk_add_f32 v[180:181], v[180:181], v[22:23] neg_lo:[0,1] neg_hi:[0,1]\n\t"
      "v_pk_add_f32 v[182:183], v[182:183], v[24:25] neg_lo:[0,1] neg_hi:[0,1]\n\t"
      "v_pk_add_f32 v[96:97], v[96:97], v[176:177]\n\t"
      "v_pk_add_f32 v[98:99], v[98:99], v[178:179]\n\t"
      "v_pk_add_f32 v[100:101], v[100:101], v[180:181]\n\t"
      "v_pk_add_f32 v[102:103], v[102:103], v[182:183]\n\t"
      "v_pk_fma_f32 v[104:105], v[176:177], v[176:177], v[104:105]\n\t"
      "v_pk_fma_f32 v[106:107], v[178:179], v[178:179], v[106:107]\n\t"
      "v_pk_fma_f32 v[108:109], v[180:181], v[180:181], v[108:109]\n\t"
      "v_pk_fma_f32 v[110:111], v[182:183], v[182:183], v[110:111]\n\t"
      "v_pk_add_f32 v[192:193], v[192:193], v[18:19] neg_lo:[0,1] neg_hi:[0,1]\n\t"
      "v_pk_add_f32 v[194:195], v[194:195], v[20:21] neg_lo:[0,1] neg_hi:[0,1]\n\t"
      "v_pk_add_f32 v[196:197], v[196:197], v[22:23] neg_lo:[0,1] neg_hi:[0,1]\n\t"
      "v_pk_add_f32 v[198:199], v[198:199], v[24:25] neg_lo:[0,1] neg_hi:[0,1]\n\t"
      "v_pk_add_f32 v[96:97], v[96:97], v[192:193]\n\t"
      "v_pk_add_f32 v[98:99], v[98:99], v[194:195]\n\t"
      "v_pk_add_f32 v[100:101], v[100:101], v[196:197]\n\t"
      "v_pk_add_f32 v[102:103], v[102:103], v[198:199]\n\t"
      "v_pk_fma_f32 v[104:105], v[192:193], v[192:193], v[104:105]\n\t"
      "v_pk_fma_f32 v[106:107], v[194:195], v[194:195], v[106:107]\n\t"
      "v_pk_fma_f32 v[108:109], v[196:197], v[196:197], v[108:109]\n\t"
      "v_pk_fma_f32 v[110:111], v[198:199], v[198:199], v[110:111]\n\t"
      "v_pk_add_f32 v[208:209], v[208:209], v[18:19] neg_lo:[0,1] neg_hi:[0,1]\n\t"
      "v_pk_add_f32 v[210:211], v[210:211], v[20:21] neg_lo:[0,1] neg_hi:[0,1]\n\t"
      "v_pk_add_f32 v[212:213], v[212:213], v[22:23] neg_lo:[0,1] neg_hi:[0,1]\n\t"
      "v_pk_add_f32 v[214:215], v[214:215], v[24:25] neg_lo:[0,1] neg_hi:[0,1]\n\t"
      "v_pk_add_f32 v[96:97], v[96:97], v[208:209]\n\t"
      "v_pk_add_f32 v[98:99], v[98:99], v[210:211]\n\t"
      "v_pk_add_f32 v[100:101], v[100:101], v[212:213]\n\t"
      "v_pk_add_f32 v[102:103], v[102:103], v[214:215]\n\t"
      "v_pk_fma_f32 v[104:105], v[208:209], v[208:209], v[104:105]\n\t"
      "v_pk_fma_f32 v[106:107], v[210:211], v[210:211], v[106:107]\n\t"
      "v_pk_fma_f32 v[108:109], v[212:213], v[212:213], v[108:109]\n\t"
      "v_pk_fma_f32 v[110:111], v[214:215], v[214:215], v[110:111]\n\t"
      "v_pk_add_f32 v[224:225], v[224:225], v[18:19] neg_lo:[0,1] neg_hi:[0,1]\n\t"
      "v_pk_add_f32 v[226:227], v[226:227], v[20:21] neg_lo:[0,1] neg_hi:[0,1]\n\t"
      "v_pk_add_f32 v[228:229], v[228:229], v[22:23] neg_lo:[0,1] neg_hi:[0,1]\n\t"
      "v_pk_add_f32 v[230:231], v[230:231], v[24:25] neg_lo:[0,1] neg_hi:[0,1]\n\t"
      "v_pk_add_f32 v[96:97], v[96:97], v[224:225]\n\t"
      "v_pk_add_f32 v[98:99], v[98:99], v[226:227]\n\t"
      "v_pk_add_f32 v[100:101], v[100:101], v[228:229]\n\t"
      "v_pk_add_f32 v[102:103], v[102:103], v[230:231]\n\t"
      "v_pk_fma_f32 v[104:105], v[224:225], v[224:225], v[104:105]\n\t"
      "v_pk_fma_f32 v[106:107], v[226:227], v[226:227], v[106:107]\n\t"
      "v_pk_fma_f32 v[108:109], v[228:229], v[228:229], v[108:109]\n\t"
      "v_pk_fma_f32 v[110:111], v[230:231], v[230:231], v[110:111]\n\t"
      "v_pk_add_f32 v[240:241], v[240:241], v[18:19] neg_lo:[0,1] neg_hi:[0,1]\n\t"
      "v_pk_add_f32 v[242:243], v[242:243], v[20:21] neg_lo:[0,1] neg_hi:[0,1]\n\t"
      "v_pk_add_f32 v[244:245], v[244:245], v[22:23] neg_lo:[0,1] neg_hi:[0,1]\n\t"
      "v_pk_add_f32 v[246:247], v[246:247], v[24:25] neg_lo:[0,1] neg_hi:[0,1]\n\t"
      "v_pk_add_f32 v[96:97], v[96:97], v[240:241]\n\t"
      "v_pk_add_f32 v[98:99], v[98:99], v[242:243]\n\t"
      "v_pk_add_f32 v[100:101], v[100:101], v[244:245]\n\t"
      "v_pk_add_f32 v[102:103], v[102:103], v[246:247]\n\t"
      "v_pk_fma_f32 v[104:105], v[240:241], v[240:241], v[104:105]\n\t"
      "v_pk_fma_f32 v[106:107], v[242:243], v[242:243], v[106:107]\n\t"
      "v_pk_fma_f32 v[108:109], v[244:245], v[244:245], v[108:109]\n\t"
      "v_pk_fma_f32 v[110:111], v[246:247], v[246:247], v[110:111]\n\t"
      "s_mov_b64 exec, -1\n\t"
      "v_add_f32_dpp v96, v96, v96 row_ror:8 row_mask:0xf bank_mask:0x3\n\t"
      "v_add_f32_dpp v98, v98, v98 row_ror:8 row_mask:0xf bank_mask:0x3\n\t"
      "v_add_f32_dpp v100, v100, v100 row_ror:8 row_mask:0xf bank_mask:0x3\n\t"
      "v_add_f32_dpp v102, v102, v102 row_ror:8 row_mask:0xf bank_mask:0x3\n\t"
      "v_add_f32_dpp v104, v104, v104 row_ror:8 row_mask:0xf bank_mask:0x3\n\t"
      "v_add_f32_dpp v106, v106, v106 row_ror:8 row_mask:0xf bank_mask:0x3\n\t"
      "v_add_f32_dpp v108, v108, v108 row_ror:8 row_mask:0xf bank_mask:0x3\n\t"
      "v_add_f32_dpp v110, v110, v110 row_ror:8 row_mask:0xf bank_mask:0x3\n\t"
      "v_add_f32_dpp v96, v97, v97 row_ror:8 row_mask:0xf bank_mask:0xc\n\t"
      "v_add_f32_dpp v98, v99, v99 row_ror:8 row_mask:0xf bank_mask:0xc\n\t"
      "v_add_f32_dpp v100, v101, v101 row_ror:8 row_mask:0xf bank_mask:0xc\n\t"
      "v_add_f32_dpp v102, v103, v103 row_ror:8 row_mask:0xf bank_mask:0xc\n\t"
      "v_add_f32_dpp v104, v105, v105 row_ror:8 row_mask:0xf bank_mask:0xc\n\t"
      "v_add_f32_dpp v106, v107, v107 row_ror:8 row_mask:0xf bank_mask:0xc\n\t"
      "v_add_f32_dpp v108, v109, v109 row_ror:8 row_mask:0xf bank_mask:0xc\n\t"
      "v_add_f32_dpp v110, v111, v111 row_ror:8 row_mask:0xf bank_mask:0xc\n\t"
      "v_add_f32_dpp v96, v96, v96 row_shl:4 row_mask:0xf bank_mask:0x5\n\t"
      "v_add_f32_dpp v100, v100, v100 row_shl:4 row_mask:0xf bank_mask:0x5\n\t"
      "v_add_f32_dpp v104, v104, v104 row_shl:4 row_mask:0xf bank_mask:0x5\n\t"
      "v_add_f32_dpp v108, v108, v108 row_shl:4 row_mask:0xf bank_mask:0x5\n\t"
      "v_add_f32_dpp v96, v98, v98 row_shr:4 row_mask:0xf bank_mask:0xa\n\t"
      "v_add_f32_dpp v100, v102, v102 row_shr:4 row_mask:0xf bank_mask:0xa\n\t"
      "v_add_f32_dpp v104, v106, v106 row_shr:4 row_mask:0xf bank_mask:0xa\n\t"
      "v_add_f32_dpp v108, v110, v110 row_shr:4 row_mask:0xf bank_mask:0xa\n\t"
      "s_mov_b32 exec_lo, 0x10001\n\t"
      "s_mov_b32 exec_hi, 0x10001\n\t"
      "ds_write_b128 v12, v[18:21] offset:0\n\t"
      "ds_write_b128 v12, v[22:25] offset:16\n\t"
      "s_mov_b64 exec, -1\n\t"
      "v_add_f32_dpp v96, v96, v96 quad_perm:[2,3,0,1] row_mask:0xf bank_mask:0xf\n\t"
      "v_add_f32_dpp v100, v100, v100 quad_perm:[2,3,0,1] row_mask:0xf bank_mask:0xf\n\t"
      "v_add_f32_dpp v104, v104, v104 quad_perm:[2,3,0,1] row_mask:0xf bank_mask:0xf\n\t"
      "v_add_f32_dpp v108, v108, v108 quad_perm:[2,3,0,1] row_mask:0xf bank_mask:0xf\n\t"
      "v_add_f32_dpp v112, v96, v96 quad_perm:[1,0,3,2] row_mask:0xf bank_mask:0xf\n\t"
      "v_add_f32_dpp v114, v100, v100 quad_perm:[1,0,3,2] row_mask:0xf bank_mask:0xf\n\t"
      "v_add_f32_dpp v113, v104, v104 quad_perm:[1,0,3,2] row_mask:0xf bank_mask:0xf\n\t"
      "v_add_f32_dpp v115, v108, v108 quad_perm:[1,0,3,2] row_mask:0xf bank_mask:0xf\n\t"
      "s_mov_b32 exec_lo, 0x11111111\n\t"
      "s_mov_b32 exec_hi, 0x11111111\n\t"
      "ds_write_b32 v13, v112 offset:0\n\t"
      "ds_write_b32 v13, v114 offset:16\n\t"
      "ds_write_b32 v13, v113 offset:1024\n\t"
      "ds_write_b32 v13, v115 offset:1040\n\t"
      "s_mov_b64 exec, 1\n\t"
      "ds_write_b32 v6, v2 offset:0\n\t"
      "s_mov_b64 exec, -1\n\t"
      "v_mfma_f32_16x16x32_bf16 v[136:139], v[44:47], v[60:63], v[136:139]\n\t"
      "ds_read_b64_tr_b16 v[52:53], v43 offset:8\n\t"
      "v_mfma_f32_16x16x32_bf16 v[136:139], v[48:51], v[64:67], v[136:139]\n\t"
      "ds_read_b64_tr_b16 v[54:55], v43 offset:1032\n\t"
      "v_mfma_f32_16x16x32_bf16 v[152:155], v[44:47], v[64:67], v[152:155]\n\t"
      "ds_read_b64_tr_b16 v[56:57], v43 offset:8200\n\t"
      "v_mfma_f32_16x16x32_bf16 v[152:155], v[48:51], v[68:71], v[152:155]\n\t"
      "ds_read_b64_tr_b16 v[58:59], v43 offset:9224\n\t"
      "v_mfma_f32_16x16x32_bf16 v[168:171], v[44:47], v[68:71], v[168:171]\n\t"
      "v_mfma_f32_16x16x32_bf16 v[168:171], v[48:51], v[72:75], v[168:171]\n\t"
      "v_mfma_f32_16x16x32_bf16 v[184:187], v[44:47], v[72:75], v[184:187]\n\t"
      "v_mfma_f32_16x16x32_bf16 v[184:187], v[48:51], v[76:79], v[184:187]\n\t"
      "v_mfma_f32_16x16x32_bf16 v[200:203], v[44:47], v[76:79], v[200:203]\n\t"
      "v_mfma_f32_16x16x32_bf16 v[200:203], v[48:51], v[80:83], v[200:203]\n\t"
      "v_mfma_f32_16x16x32_bf16 v[216:219], v[44:47], v[80:83], v[216:219]\n\t"
      "v_add_u32 v43, 32768, v5\n\t"
      "v_mfma_f32_16x16x32_bf16 v[216:219], v[48:51], v[84:87], v[216:219]\n\t"
      "v_add_u32 v42, 65536, v9\n\t"
      "v_mfma_f32_16x16x32_bf16 v[232:235], v[44:47], v[84:87], v[232:235]\n\t"
      "v_add_u32 v41, 65536, v11\n\t"
      "v_mfma_f32_16x16x32_bf16 v[232:235], v[48:51], v[88:91], v[232:235]\n\t"
      "v_mfma_f32_16x16x32_bf16 v[248:251], v[44:47], v[88:91], v[248:251]\n\t"
      "v_mfma_f32_16x16x32_bf16 v[248:251], v[48:51], v[92:95], v[248:251]\n\t"
      "s_waitcnt lgkmcnt(0)\n\t"
      "v_mfma_f32_16x16x32_bf16 v[140:143], v[52:55], v[60:63], v[140:143]\n\t"
      "ds_read_b64_tr_b16 v[44:45], v43 offset:0\n\t"
      "v_mfma_f32_16x16x32_bf16 v[140:143], v[56:59], v[64:67], v[140:143]\n\t"
      "ds_read_b64_tr_b16 v[46:47], v43 offset:1024\n\t"
      "v_mfma_f32_16x16x32_bf16 v[156:159], v[52:55], v[64:67], v[156:159]\n\t"
      "ds_read_b128 v[96:99], v42 offset:0\n\t"
      "v_mfma_f32_16x16x32_bf16 v[156:159], v[56:59], v[68:71], v[156:159]\n\t"
      "ds_read_b64_tr_b16 v[48:49], v43 offset:8192\n\t"
      "v_mfma_f32_16x16x32_bf16 v[172:175], v[52:55], v[68:71], v[172:175]\n\t"
      "ds_read_b64_tr_b16 v[50:51], v43 offset:9216\n\t"
      "v_mfma_f32_16x16x32_bf16 v[172:175], v[56:59], v[72:75], v[172:175]\n\t"
      "ds_read_b128 v[100:103], v42 offset:4096\n\t"
      "v_mfma_f32_16x16x32_bf16 v[188:191], v[52:55], v[72:75], v[188:191]\n\t"
      "ds_read_b128 v[104:107], v42 offset:8192\n\t"
      "v_mfma_f32_16x16x32_bf16 v[188:191], v[56:59], v[76:79], v[188:191]\n\t"
      "ds_read_b128 v[108:111], v42 offset:12288\n\t"
      "v_mfma_f32_16x16x32_bf16 v[204:207], v[52:55], v[76:79], v[204:207]\n\t"
      "ds_read_b128 v[112:115], v42 offset:16384\n\t"
      "v_mfma_f32_16x16x32_bf16 v[204:207], v[56:59], v[80:83], v[204:207]\n\t"
      "ds_read_b128 v[116:119], v42 offset:20480\n\t"
      "v_mfma_f32_16x16x32_bf16 v[220:223], v[52:55], v[80:83], v[220:223]\n\t"
      "ds_read_b128 v[120:123], v42 offset:24576\n\t"
      "v_mfma_f32_16x16x32_bf16 v[220:223], v[56:59], v[84:87], v[220:223]\n\t"
      "ds_read_b128 v[124:127], v42 offset:28672\n\t"
      "v_mfma_f32_16x16x32_bf16 v[236:239], v[52:55], v[84:87], v[236:239]\n\t"
      "ds_read_b128 v[60:63], v41\n\t"
      "v_mfma_f32_16x16x32_bf16 v[236:239], v[56:59], v[88:91], v[236:239]\n\t"
      "v_mfma_f32_16x16x32_bf16 v[252:255], v[52:55], v[88:91], v[252:255]\n\t"
      "v_mfma_f32_16x16x32_bf16 v[252:255], v[56:59], v[92:95], v[252:255]\n\t"
      "s_waitcnt lgkmcnt(7)\n\t"
      "v_mfma_f32_16x16x32_bf16 v[136:139], v[44:47], v[96:99], v[136:139]\n\t"
      "ds_read_b64_tr_b16 v[52:53], v43 offset:8\n\t"
      "v_mfma_f32_16x16x32_bf16 v[136:139], v[48:51], v[100:103], v[136:139]\n\t"
      "ds_read_b64_tr_b16 v[54:55], v43 offset:1032\n\t"
      "s_waitcnt lgkmcnt(8)\n\t"
      "v_mfma_f32_16x16x32_bf16 v[152:155], v[44:47], v[100:103], v[152:155]\n\t"
      "ds_read_b64_tr_b16 v[56:57], v43 offset:8200\n\t"
      "v_mfma_f32_16x16x32_bf16 v[152:155], v[48:51], v[104:107], v[152:155]\n\t"
      "ds_read_b64_tr_b16 v[58:59], v43 offset:9224\n\t"
      "s_waitcnt lgkmcnt(9)\n\t"
      "v_mfma_f32_16x16x32_bf16 v[168:171], v[44:47], v[104:107], v[168:171]\n\t"
      "v_mfma_f32_16x16x32_bf16 v[168:171], v[48:51], v[108:111], v[168:171]\n\t"
      "s_waitcnt lgkmcnt(8)\n\t"
      "v_mfma_f32_16x16x32_bf16 v[184:187], v[44:47], v[108:111], v[184:187]\n\t"
      "v_mfma_f32_16x16x32_bf16 v[184:187], v[48:51], v[112:115], v[184:187]\n\t"
      "s_waitcnt lgkmcnt(7)\n\t"
      "v_mfma_f32_16x16x32_bf16 v[200:203], v[44:47], v[112:115], v[200:203]\n\t"
      "v_mfma_f32_16x16x32_bf16 v[200:203], v[48:51], v[116:119], v[200:203]\n\t"
      "s_waitcnt lgkmcnt(6)\n\t"
      "v_mfma_f32_16x16x32_bf16 v[216:219], v[44:47], v[116:119], v[216:219]\n\t"
      "v_add_u32 v43, 49152, v5\n\t"
      "v_mfma_f32_16x16x32_bf16 v[216:219], v[48:51], v[120:123], v[216:219]\n\t"
      "v_add_u32 v42, 0, v8\n\t"
      "s_waitcnt lgkmcnt(5)\n\t"
      "v_mfma_f32_16x16x32_bf16 v[232:235], v[44:47], v[120:123], v[232:235]\n\t"
      "v_add_u32 v41, 0, v9\n\t"
      "v_mfma_f32_16x16x32_bf16 v[232:235], v[48:51], v[124:127], v[232:235]\n\t"
      "s_waitcnt lgkmcnt(4)\n\t"
      "v_mfma_f32_16x16x32_bf16 v[248:251], v[44:47], v[124:127], v[248:251]\n\t"
      "v_mfma_f32_16x16x32_bf16 v[248:251], v[48:51], v[60:63], v[248:251]\n\t"
      "s_waitcnt lgkmcnt(0)\n\t"
      "v_mfma_f32_16x16x32_bf16 v[140:143], v[52:55], v[96:99], v[140:143]\n\t"
      "ds_read_b64_tr_b16 v[44:45], v43 offset:0\n\t"
      "v_mfma_f32_16x16x32_bf16 v[140:143], v[56:59], v[100:103], v[140:143]\n\t"
      "ds_read_b64_tr_b16 v[46:47], v43 offset:1024\n\t"
      "v_mfma_f32_16x16x32_bf16 v[156:159], v[52:55], v[100:103], v[156:159]\n\t"
      "ds_read_b128 v[64:67], v42 offset:0\n\t"
      "v_mfma_f32_16x16x32_bf16 v[156:159], v[56:59], v[104:107], v[156:159]\n\t"
      "ds_read_b64_tr_b16 v[48:49], v43 offset:8192\n\t"
      "v_mfma_f32_16x16x32_bf16 v[172:175], v[52:55], v[104:107], v[172:175]\n\t"
      "ds_read_b64_tr_b16 v[50:51], v43 offset:9216\n\t"
      "v_mfma_f32_16x16x32_bf16 v[172:175], v[56:59], v[108:111], v[172:175]\n\t"
      "ds_read_b128 v[68:71], v41 offset:0\n\t"
      "v_mfma_f32_16x16x32_bf16 v[188:191], v[52:55], v[108:111], v[188:191]\n\t"
      "ds_read_b128 v[72:75], v41 offset:4096\n\t"
      "v_mfma_f32_16x16x32_bf16 v[188:191], v[56:59], v[112:115], v[188:191]\n\t"
      "ds_read_b128 v[76:79], v42 offset:4096\n\t"
      "v_mfma_f32_16x16x32_bf16 v[204:207], v[52:55], v[112:115], v[204:207]\n\t"
      "ds_read_b128 v[80:83], v42 offset:8192\n\t"
      "v_mfma_f32_16x16x32_bf16 v[204:207], v[56:59], v[116:119], v[204:207]\n\t"
      "ds_read_b128 v[84:87], v41 offset:8192\n\t"
      "v_mfma_f32_16x16x32_bf16 v[220:223], v[52:55], v[116:119], v[220:223]\n\t"
      "ds_read_b128 v[88:91], v41 offset:12288\n\t"
      "v_mfma_f32_16x16x32_bf16 v[220:223], v[56:59], v[120:123], v[220:223]\n\t"
      "ds_read_b128 v[92:95], v42 offset:12288\n\t"
      "v_mfma_f32_16x16x32_bf16 v[236:239], v[52:55], v[120:123], v[236:239]\n\t"
      "ds_read_b128 v[96:99], v42 offset:16384\n\t"
      "v_mfma_f32_16x16x32_bf16 v[236:239], v[56:59], v[124:127], v[236:239]\n\t"
      "ds_read_b128 v[100:103], v41 offset:16384\n\t"
      "v_mfma_f32_16x16x32_bf16 v[252:255], v[52:55], v[124:127], v[252:255]\n\t"
      "ds_read_b128 v[104:107], v41 offset:20480\n\t"
      "v_mfma_f32_16x16x32_bf16 v[252:255], v[56:59], v[60:63], v[252:255]\n\t"
      "s_waitcnt lgkmcnt(7)\n\t"
      "ds_read_b128 v[108:111], v42 offset:20480\n\t"
      "v_mfma_f32_16x16x32_bf16 v[136:139], v[44:47], v[64:67], v[136:139]\n\t"
      "ds_read_b128 v[112:115], v42 offset:24576\n\t"
      "v_mfma_f32_16x16x32_bf16 v[136:139], v[48:51], v[68:71], v[136:139]\n\t"
      "ds_read_b128 v[116:119], v41 offset:24576\n\t"
      "v_mfma_f32_16x16x32_bf16 v[152:155], v[48:51], v[72:75], v[152:155]\n\t"
      "ds_read_b128 v[120:123], v41 offset:28672\n\t"
      "v_mfma_f32_16x16x32_bf16 v[152:155], v[44:47], v[76:79], v[152:155]\n\t"
      "ds_read_b128 v[124:127], v42 offset:28672\n\t"
      "s_waitcnt lgkmcnt(10)\n\t"
      "v_mfma_f32_16x16x32_bf16 v[168:171], v[44:47], v[80:83], v[168:171]\n\t"
      "ds_read_b64_tr_b16 v[52:53], v43 offset:8\n\t"
      "v_mfma_f32_16x16x32_bf16 v[168:171], v[48:51], v[84:87], v[168:171]\n\t"
      "ds_read_b64_tr_b16 v[54:55], v43 offset:1032\n\t"
      "s_waitcnt lgkmcnt(10)\n\t"
      "v_mfma_f32_16x16x32_bf16 v[184:187], v[48:51], v[88:91], v[184:187]\n\t"
      "ds_read_b64_tr_b16 v[56:57], v43 offset:8200\n\t"
      "v_mfma_f32_16x16x32_bf16 v[184:187], v[44:47], v[92:95], v[184:187]\n\t"
      "ds_read_b64_tr_b16 v[58:59], v43 offset:9224\n\t"
      "s_waitcnt lgkmcnt(10)\n\t"
      "v_mfma_f32_16x16x32_bf16 v[200:203], v[44:47], v[96:99], v[200:203]\n\t"
      "v_mfma_f32_16x16x32_bf16 v[200:203], v[48:51], v[100:103], v[200:203]\n\t"
      "s_waitcnt lgkmcnt(8)\n\t"
      "v_mfma_f32_16x16x32_bf16 v[216:219], v[48:51], v[104:107], v[216:219]\n\t"
      "v_mfma_f32_16x16x32_bf16 v[216:219], v[44:47], v[108:111], v[216:219]\n\t"
      "s_waitcnt lgkmcnt(6)\n\t"
      "v_mfma_f32_16x16x32_bf16 v[232:235], v[44:47], v[112:115], v[232:235]\n\t"
      "v_mfma_f32_16x16x32_bf16 v[232:235], v[48:51], v[116:119], v[232:235]\n\t"
      "s_waitcnt lgkmcnt(4)\n\t"
      "v_mfma_f32_16x16x32_bf16 v[248:251], v[48:51], v[120:123], v[248:251]\n\t"
      "v_mfma_f32_16x16x32_bf16 v[248:251], v[44:47], v[124:127], v[248:251]\n\t"
      "s_waitcnt lgkmcnt(0)\n\t"
      "v_mfma_f32_16x16x32_bf16 v[140:143], v[52:55], v[64:67], v[140:143]\n\t"
      "v_mfma_f32_16x16x32_bf16 v[140:143], v[56:59], v[68:71], v[140:143]\n\t"
      "v_mfma_f32_16x16x32_bf16 v[156:159], v[56:59], v[72:75], v[156:159]\n\t"
      "v_mfma_f32_16x16x32_bf16 v[156:159], v[52:55], v[76:79], v[156:159]\n\t"
      "v_mfma_f32_16x16x32_bf16 v[172:175], v[52:55], v[80:83], v[172:175]\n\t"
      "v_mfma_f32_16x16x32_bf16 v[172:175], v[56:59], v[84:87], v[172:175]\n\t"
      "v_mov_b32_dpp v26, v136 row_newbcast:0 row_mask:0xf bank_mask:0xf\n\t"
      "v_mfma_f32_16x16x32_bf16 v[188:191], v[56:59], v[88:91], v[188:191]\n\t"
      "v_mov_b32_dpp v27, v137 row_newbcast:0 row_mask:0xf bank_mask:0xf\n\t"
      "v_mfma_f32_16x16x32_bf16 v[188:191], v[52:55], v[92:95], v[188:191]\n\t"
      "v_mov_b32_dpp v28, v138 row_newbcast:0 row_mask:0xf bank_mask:0xf\n\t"
      "v_mfma_f32_16x16x32_bf16 v[204:207], v[52:55], v[96:99], v[204:207]\n\t"
      "v_mov_b32_dpp v29, v139 row_newbcast:0 row_mask:0xf bank_mask:0xf\n\t"
      "v_mfma_f32_16x16x32_bf16 v[204:207], v[56:59], v[100:103], v[204:207]\n\t"
      "v_mfma_f32_16x16x32_bf16 v[220:223], v[56:59], v[104:107], v[220:223]\n\t"
      "v_mov_b32_dpp v30, v140 row_newbcast:0 row_mask:0xf bank_mask:0xf\n\t"
      "v_mfma_f32_16x16x32_bf16 v[220:223], v[52:55], v[108:111], v[220:223]\n\t"
      "v_mov_b32_dpp v31, v141 row_newbcast:0 row_mask:0xf bank_mask:0xf\n\t"
      "v_mfma_f32_16x16x32_bf16 v[236:239], v[52:55], v[112:115], v[236:239]\n\t"
      "v_mov_b32_dpp v32, v142 row_newbcast:0 row_mask:0xf bank_mask:0xf\n\t"
      "v_mfma_f32_16x16x32_bf16 v[236:239], v[56:59], v[116:119], v[236:239]\n\t"
      "v_mov_b32_dpp v33, v143 row_newbcast:0 row_mask:0xf bank_mask:0xf\n\t"
      "v_mfma_f32_16x16x32_bf16 v[252:255], v[56:59], v[120:123], v[252:255]\n\t"
      "v_mfma_f32_16x16x32_bf16 v[252:255], v[52:55], v[124:127], v[252:255]\n\t"
      "v_pk_add_f32 v[96:97], v[136:137], v[26:27] neg_lo:[0,1] neg_hi:[0,1]\n\t"
      "v_pk_add_f32 v[98:99], v[138:139], v[28:29] neg_lo:[0,1] neg_hi:[0,1]\n\t"
      "v_pk_add_f32 v[100:101], v[140:141], v[30:31] neg_lo:[0,1] neg_hi:[0,1]\n\t"
      "v_pk_add_f32 v[102:103], v[142:143], v[32:33] neg_lo:[0,1] neg_hi:[0,1]\n\t"
      "v_pk_add_f32 v[136:137], v[136:137], v[26:27] neg_lo:[0,1] neg_hi:[0,1]\n\t"
      "v_pk_add_f32 v[138:139], v[138:139], v[28:29] neg_lo:[0,1] neg_hi:[0,1]\n\t"
      "v_pk_add_f32 v[140:141], v[140:141], v[30:31] neg_lo:[0,1] neg_hi:[0,1]\n\t"
      "v_pk_add_f32 v[142:143], v[142:143], v[32:33] neg_lo:[0,1] neg_hi:[0,1]\n\t"
      "v_pk_mul_f32 v[104:105], v[96:97], v[96:97]\n\t"
      "v_pk_mul_f32 v[106:107], v[98:99], v[98:99]\n\t"
      "v_pk_mul_f32 v[108:109], v[100:101], v[100:101]\n\t"
      "v_pk_mul_f32 v[110:111], v[102:103], v[102:103]\n\t"
      "s_mov_b64 exec, s[56:57]\n\t"
      "v_pk_add_f32 v[152:153], v[152:153], v[26:27] neg_lo:[0,1] neg_hi:[0,1]\n\t"
      "v_pk_add_f32 v[154:155], v[154:155], v[28:29] neg_lo:[0,1] neg_hi:[0,1]\n\t"
      "v_pk_add_f32 v[156:157], v[156:157], v[30:31] neg_lo:[0,1] neg_hi:[0,1]\n\t"
      "v_pk_add_f32 v[158:159], v[158:159], v[32:33] neg_lo:[0,1] neg_hi:[0,1]\n\t"
      "v_pk_add_f32 v[96:97], v[96:97], v[152:153]\n\t"
      "v_pk_add_f32 v[98:99], v[98:99], v[154:155]\n\t"
      "v_pk_add_f32 v[100:101], v[100:101], v[156:157]\n\t"
      "v_pk_add_f32 v[102:103], v[102:103], v[158:159]\n\t"
      "v_pk_fma_f32 v[104:105], v[152:153], v[152:153], v[104:105]\n\t"
      "v_pk_fma_f32 v[106:107], v[154:155], v[154:155], v[106:107]\n\t"
      "v_pk_fma_f32 v[108:109], v[156:157], v[156:157], v[108:109]\n\t"
      "v_pk_fma_f32 v[110:111], v[158:159], v[158:159], v[110:111]\n\t"
      "s_mov_b64 exec, s[58:59]\n\t"
      "v_pk_add_f32 v[168:169], v[168:169], v[26:27] neg_lo:[0,1] neg_hi:[0,1]\n\t"
      "v_pk_add_f32 v[170:171], v[170:171], v[28:29] neg_lo:[0,1] neg_hi:[0,1]\n\t"
      "v_pk_add_f32 v[172:173], v[172:173], v[30:31] neg_lo:[0,1] neg_hi:[0,1]\n\t"
      "v_pk_add_f32 v[174:175], v[174:175], v[32:33] neg_lo:[0,1] neg_hi:[0,1]\n\t"
      "v_pk_add_f32 v[96:97], v[96:97], v[168:169]\n\t"
      "v_pk_add_f32 v[98:99], v[98:99], v[170:171]\n\t"
      "v_pk_add_f32 v[100:101], v[100:101], v[172:173]\n\t"
      "v_pk_add_f32 v[102:103], v[102:103], v[174:175]\n\t"
      "v_pk_fma_f32 v[104:105], v[168:169], v[168:169], v[104:105]\n\t"
      "v_pk_fma_f32 v[106:107], v[170:171], v[170:171], v[106:107]\n\t"
      "v_pk_fma_f32 v[108:109], v[172:173], v[172:173], v[108:109]\n\t"
      "v_pk_fma_f32 v[110:111], v[174:175], v[174:175], v[110:111]\n\t"
      "v_pk_add_f32 v[184:185], v[184:185], v[26:27] neg_lo:[0,1] neg_hi:[0,1]\n\t"
      "v_pk_add_f32 v[186:187], v[186:187], v[28:29] neg_lo:[0,1] neg_hi:[0,1]\n\t"
      "v_pk_add_f32 v[188:189], v[188:189], v[30:31] neg_lo:[0,1] neg_hi:[0,1]\n\t"
      "v_pk_add_f32 v[190:191], v[190:191], v[32:33] neg_lo:[0,1] neg_hi:[0,1]\n\t"
      "v_pk_add_f32 v[96:97], v[96:97], v[184:185]\n\t"
      "v_pk_add_f32 v[98:99], v[98:99], v[186:187]\n\t"
      "v_pk_add_f32 v[100:101], v[100:101], v[188:189]\n\t"
      "v_pk_add_f32 v[102:103], v[102:103], v[190:191]\n\t"
      "v_pk_fma_f32 v[104:105], v[184:185], v[184:185], v[104:105]\n\t"
      "v_pk_fma_f32 v[106:107], v[186:187], v[186:187], v[106:107]\n\t"
      "v_pk_fma_f32 v[108:109], v[188:189], v[188:189], v[108:109]\n\t"
      "v_pk_fma_f32 v[110:111], v[190:191], v[190:191], v[110:111]\n\t"
      "v_pk_add_f32 v[200:201], v[200:201], v[26:27] neg_lo:[0,1] neg_hi:[0,1]\n\t"
      "v_pk_add_f32 v[202:203], v[202:203], v[28:29] neg_lo:[0,1] neg_hi:[0,1]\n\t"
      "v_pk_add_f32 v[204:205], v[204:205], v[30:31] neg_lo:[0,1] neg_hi:[0,1]\n\t"
      "v_pk_add_f32 v[206:207], v[206:207], v[32:33] neg_lo:[0,1] neg_hi:[0,1]\n\t"
      "v_pk_add_f32 v[96:97], v[96:97], v[200:201]\n\t"
      "v_pk_add_f32 v[98:99], v[98:99], v[202:203]\n\t"
      "v_pk_add_f32 v[100:101], v[100:101], v[204:205]\n\t"
      "v_pk_add_f32 v[102:103], v[102:103], v[206:207]\n\t"
      "v_pk_fma_f32 v[104:105], v[200:201], v[200:201], v[104:105]\n\t"
      "v_pk_fma_f32 v[106:107], v[202:203], v[202:203], v[106:107]\n\t"
      "v_pk_fma_f32 v[108:109], v[204:205], v[204:205], v[108:109]\n\t"
      "v_pk_fma_f32 v[110:111], v[206:207], v[206:207], v[110:111]\n\t"
      "v_pk_add_f32 v[216:217], v[216:217], v[26:27] neg_lo:[0,1] neg_hi:[0,1]\n\t"
      "v_pk_add_f32 v[218:219], v[218:219], v[28:29] neg_lo:[0,1] neg_hi:[0,1]\n\t"
      "v_pk_add_f32 v[220:221], v[220:221], v[30:31] neg_lo:[0,1] neg_hi:[0,1]\n\t"
      "v_pk_add_f32 v[222:223], v[222:223], v[32:33] neg_lo:[0,1] neg_hi:[0,1]\n\t"
      "v_pk_add_f32 v[96:97], v[96:97], v[216:217]\n\t"
      "v_pk_add_f32 v[98:99], v[98:99], v[218:219]\n\t"
      "v_pk_add_f32 v[100:101], v[100:101], v[220:221]\n\t"
      "v_pk_add_f32 v[102:103], v[102:103], v[222:223]\n\t"
      "v_pk_fma_f32 v[104:105], v[216:217], v[216:217], v[104:105]\n\t"
      "v_pk_fma_f32 v[106:107], v[218:219], v[218:219], v[106:107]\n\t"
      "v_pk_fma_f32 v[108:109], v[220:221], v[220:221], v[108:109]\n\t"
      "v_pk_fma_f32 v[110:111], v[222:223], v[222:223], v[110:111]\n\t"
      "v_pk_add_f32 v[232:233], v[232:233], v[26:27] neg_lo:[0,1] neg_hi:[0,1]\n\t"
      "v_pk_add_f32 v[234:235], v[234:235], v[28:29] neg_lo:[0,1] neg_hi:[0,1]\n\t"
      "v_pk_add_f32 v[236:237], v[236:237], v[30:31] neg_lo:[0,1] neg_hi:[0,1]\n\t"
      "v_pk_add_f32 v[238:239], v[238:239], v[32:33] neg_lo:[0,1] neg_hi:[0,1]\n\t"
      "v_pk_add_f32 v[96:97], v[96:97], v[232:233]\n\t"
      "v_pk_add_f32 v[98:99], v[98:99], v[234:235]\n\t"
      "v_pk_add_f32 v[100:101], v[100:101], v[236:237]\n\t"
      "v_pk_add_f32 v[102:103], v[102:103], v[238:239]\n\t"
      "v_pk_fma_f32 v[104:105], v[232:233], v[232:233], v[104:105]\n\t"
      "v_pk_fma_f32 v[106:107], v[234:235], v[234:235], v[106:107]\n\t"
      "v_pk_fma_f32 v[108:109], v[236:237], v[236:237], v[108:109]\n\t"
      "v_pk_fma_f32 v[110:111], v[238:239], v[238:239], v[110:111]\n\t"
      "v_pk_add_f32 v[248:249], v[248:249], v[26:27] neg_lo:[0,1] neg_hi:[0,1]\n\t"
      "v_pk_add_f32 v[250:251], v[250:251], v[28:29] neg_lo:[0,1] neg_hi:[0,1]\n\t"
      "v_pk_add_f32 v[252:253], v[252:253], v[30:31] neg_lo:[0,1] neg_hi:[0,1]\n\t"
      "v_pk_add_f32 v[254:255], v[254:255], v[32:33] neg_lo:[0,1] neg_hi:[0,1]\n\t"
      "v_pk_add_f32 v[96:97], v[96:97], v[248:249]\n\t"
      "v_pk_add_f32 v[98:99], v[98:99], v[250:251]\n\t"
      "v_pk_add_f32 v[100:101], v[100:101], v[252:253]\n\t"
      "v_pk_add_f32 v[102:103], v[102:103], v[254:255]\n\t"
      "v_pk_fma_f32 v[104:105], v[248:249], v[248:249], v[104:105]\n\t"
      "v_pk_fma_f32 v[106:107], v[250:251], v[250:251], v[106:107]\n\t"
      "v_pk_fma_f32 v[108:109], v[252:253], v[252:253], v[108:109]\n\t"
      "v_pk_fma_f32 v[110:111], v[254:255], v[254:255], v[110:111]\n\t"
      "s_mov_b64 exec, -1\n\t"
      "ds_read_b32 v116, v7 offset:0\n\t"
      "ds_read_b128 v[80:83], v14 offset:0\n\t"
      "ds_read_b128 v[84:87], v14 offset:16\n\t"
      "ds_read_b128 v[88:91], v14 offset:512\n\t"
      "ds_read_b128 v[92:95], v14 offset:528\n\t"
      "v_add_f32_dpp v96, v96, v96 row_ror:8 row_mask:0xf bank_mask:0x3\n\t"
      "v_add_f32_dpp v98, v98, v98 row_ror:8 row_mask:0xf bank_mask:0x3\n\t"
      "v_add_f32_dpp v100, v100, v100 row_ror:8 row_mask:0xf bank_mask:0x3\n\t"
      "v_add_f32_dpp v102, v102, v102 row_ror:8 row_mask:0xf bank_mask:0x3\n\t"
      "v_add_f32_dpp v104, v104, v104 row_ror:8 row_mask:0xf bank_mask:0x3\n\t"
      "v_add_f32_dpp v106, v106, v106 row_ror:8 row_mask:0xf bank_mask:0x3\n\t"
      "v_add_f32_dpp v108, v108, v108 row_ror:8 row_mask:0xf bank_mask:0x3\n\t"
      "v_add_f32_dpp v110, v110, v110 row_ror:8 row_mask:0xf bank_mask:0x3\n\t"
      "v_add_f32_dpp v96, v97, v97 row_ror:8 row_mask:0xf bank_mask:0xc\n\t"
      "v_add_f32_dpp v98, v99, v99 row_ror:8 row_mask:0xf bank_mask:0xc\n\t"
      "v_add_f32_dpp v100, v101, v101 row_ror:8 row_mask:0xf bank_mask:0xc\n\t"
      "v_add_f32_dpp v102, v103, v103 row_ror:8 row_mask:0xf bank_mask:0xc\n\t"
      "v_add_f32_dpp v104, v105, v105 row_ror:8 row_mask:0xf bank_mask:0xc\n\t"
      "v_add_f32_dpp v106, v107, v107 row_ror:8 row_mask:0xf bank_mask:0xc\n\t"
      "v_add_f32_dpp v108, v109, v109 row_ror:8 row_mask:0xf bank_mask:0xc\n\t"
      "v_add_f32_dpp v110, v111, v111 row_ror:8 row_mask:0xf bank_mask:0xc\n\t"
      "v_add_f32_dpp v96, v96, v96 row_shl:4 row_mask:0xf bank_mask:0x5\n\t"
      "v_add_f32_dpp v100, v100, v100 row_shl:4 row_mask:0xf bank_mask:0x5\n\t"
      "v_add_f32_dpp v104, v104, v104 row_shl:4 row_mask:0xf bank_mask:0x5\n\t"
      "v_add_f32_dpp v108, v108, v108 row_shl:4 row_mask:0xf bank_mask:0x5\n\t"
      "v_add_f32_dpp v96, v98, v98 row_shr:4 row_mask:0xf bank_mask:0xa\n\t"
      "v_add_f32_dpp v100, v102, v102 row_shr:4 row_mask:0xf bank_mask:0xa\n\t"
      "v_add_f32_dpp v104, v106, v106 row_shr:4 row_mask:0xf bank_mask:0xa\n\t"
      "v_add_f32_dpp v108, v110, v110 row_shr:4 row_mask:0xf bank_mask:0xa\n\t"
      "s_mov_b32 exec_lo, 0x10001\n\t"
      "s_mov_b32 exec_hi, 0x10001\n\t"
      "ds_write_b128 v12, v[26:29] offset:128\n\t"
      "ds_write_b128 v12, v[30:33] offset:144\n\t"
      "s_mov_b64 exec, -1\n\t"
      "v_add_f32_dpp v96, v96, v96 quad_perm:[2,3,0,1] row_mask:0xf bank_mask:0xf\n\t"
      "v_add_f32_dpp v100, v100, v100 quad_perm:[2,3,0,1] row_mask:0xf bank_mask:0xf\n\t"
      "v_add_f32_dpp v104, v104, v104 quad_perm:[2,3,0,1] row_mask:0xf bank_mask:0xf\n\t"
      "v_add_f32_dpp v108, v108, v108 quad_perm:[2,3,0,1] row_mask:0xf bank_mask:0xf\n\t"
      "v_add_f32_dpp v112, v96, v96 quad_perm:[1,0,3,2] row_mask:0xf bank_mask:0xf\n\t"
      "v_add_f32_dpp v114, v100, v100 quad_perm:[1,0,3,2] row_mask:0xf bank_mask:0xf\n\t"
      "v_add_f32_dpp v113, v104, v104 quad_perm:[1,0,3,2] row_mask:0xf bank_mask:0xf\n\t"
      "v_add_f32_dpp v115, v108, v108 quad_perm:[1,0,3,2] row_mask:0xf bank_mask:0xf\n\t"
      "s_mov_b32 exec_lo, 0x11111111\n\t"
      "s_mov_b32 exec_hi, 0x11111111\n\t"
      "ds_write_b32 v13, v112 offset:128\n\t"
      "ds_write_b32 v13, v114 offset:144\n\t"
      "ds_write_b32 v13, v113 offset:1152\n\t"
      "ds_write_b32 v13, v115 offset:1168\n\t"
      "s_mov_b64 exec, 1\n\t"
      "ds_write_b32 v6, v2 offset:16\n\t"
      "s_mov_b64 exec, -1\n\t"
      "s_waitcnt lgkmcnt(7)\n\t"
      "v_cmp_eq_u32 vcc, 0, v116\n\t"
      "s_cbranch_vccz conv_go0\n\t"
      "conv_wait0:\n\t"
      "ds_read_b32 v116, v7 offset:0\n\t"
      "ds_read_b128 v[80:83], v14 offset:0\n\t"
      "ds_read_b128 v[84:87], v14 offset:16\n\t"
      "ds_read_b128 v[88:91], v14 offset:512\n\t"
      "ds_read_b128 v[92:95], v14 offset:528\n\t"
      "s_waitcnt lgkmcnt(4)\n\t"
      "v_cmp_eq_u32 vcc, 0, v116\n\t"
      "s_waitcnt lgkmcnt(0)\n\t"
      "s_cbranch_vccnz conv_wait0\n\t"
      "conv_go0:\n\t"
      "v_pk_fma_f32 v[88:89], v[18:19], v[80:81], v[88:89]\n\t"
      "v_pk_fma_f32 v[90:91], v[20:21], v[82:83], v[90:91]\n\t"
      "v_pk_fma_f32 v[92:93], v[22:23], v[84:85], v[92:93]\n\t"
      "v_pk_fma_f32 v[94:95], v[24:25], v[86:87], v[94:95]\n\t"
      "v_pk_fma_f32 v[128:129], v[128:129], v[80:81], v[88:89]\n\t"
      "v_pk_fma_f32 v[130:131], v[130:131], v[82:83], v[90:91]\n\t"
      "v_pk_fma_f32 v[132:133], v[132:133], v[84:85], v[92:93]\n\t"
      "v_pk_fma_f32 v[134:135], v[134:135], v[86:87], v[94:95]\n\t"
      "v_pk_fma_f32 v[44:45], v[128:129], v[128:129], v[34:35] neg_lo:[1,0,0] neg_hi:[1,0,0]\n\t"
      "v_pk_fma_f32 v[46:47], v[130:131], v[130:131], v[34:35] neg_lo:[1,0,0] neg_hi:[1,0,0]\n\t"
      "v_pk_fma_f32 v[48:49], v[132:133], v[132:133], v[34:35] neg_lo:[1,0,0] neg_hi:[1,0,0]\n\t"
      "v_pk_fma_f32 v[50:51], v[134:135], v[134:135], v[34:35] neg_lo:[1,0,0] neg_hi:[1,0,0]\n\t"
      "v_pk_mul_f32 v[44:45], v[44:45], v[128:129]\n\t"
      "v_pk_mul_f32 v[46:47], v[46:47], v[130:131]\n\t"
      "v_pk_mul_f32 v[48:49], v[48:49], v[132:133]\n\t"
      "v_pk_mul_f32 v[50:51], v[50:51], v[134:135]\n\t"
      "v_exp_f32 v44, v44\n\t"
      "v_exp_f32 v45, v45\n\t"
      "v_exp_f32 v46, v46\n\t"
      "v_exp_f32 v47, v47\n\t"
      "v_exp_f32 v48, v48\n\t"
      "v_exp_f32 v49, v49\n\t"
      "v_exp_f32 v50, v50\n\t"
      "v_exp_f32 v51, v51\n\t"
      "v_pk_fma_f32 v[44:45], v[44:45], v[36:37], v[36:37]\n\t"
      "v_pk_fma_f32 v[46:47], v[46:47], v[36:37], v[36:37]\n\t"
      "v_pk_fma_f32 v[48:49], v[48:49], v[36:37], v[36:37]\n\t"
      "v_pk_fma_f32 v[50:51], v[50:51], v[36:37], v[36:37]\n\t"
      "v_rcp_f32 v44, v44\n\t"
      "v_rcp_f32 v45, v45\n\t"
      "v_rcp_f32 v46, v46\n\t"
      "v_rcp_f32 v47, v47\n\t"
      "v_rcp_f32 v48, v48\n\t"
      "v_rcp_f32 v49, v49\n\t"
      "v_rcp_f32 v50, v50\n\t"
      "v_rcp_f32 v51, v51\n\t"
      "v_pk_mul_f32 v[44:45], v[44:45], v[128:129]\n\t"
      "v_pk_mul_f32 v[46:47], v[46:47], v[130:131]\n\t"
      "v_pk_mul_f32 v[48:49], v[48:49], v[132:133]\n\t"
      "v_pk_mul_f32 v[50:51], v[50:51], v[134:135]\n\t"
      "v_cvt_pk_bf16_f32 v52, v44, v45\n\t"
      "v_cvt_pk_bf16_f32 v53, v46, v47\n\t"
      "v_cvt_pk_bf16_f32 v54, v48, v49\n\t"
      "v_cvt_pk_bf16_f32 v55, v50, v51\n\t"
      "buffer_store_dwordx4 v[52:55], v15, s[24:27], 0 offen offset:0 nt\n\t"
      "v_pk_fma_f32 v[144:145], v[144:145], v[80:81], v[88:89]\n\t"
      "v_pk_fma_f32 v[146:147], v[146:147], v[82:83], v[90:91]\n\t"
      "v_pk_fma_f32 v[148:149], v[148:149], v[84:85], v[92:93]\n\t"
      "v_pk_fma_f32 v[150:151], v[150:151], v[86:87], v[94:95]\n\t"
      "v_pk_fma_f32 v[44:45], v[144:145], v[144:145], v[34:35] neg_lo:[1,0,0] neg_hi:[1,0,0]\n\t"
      "v_pk_fma_f32 v[46:47], v[146:147], v[146:147], v[34:35] neg_lo:[1,0,0] neg_hi:[1,0,0]\n\t"
      "v_pk_fma_f32 v[48:49], v[148:149], v[148:149], v[34:35] neg_lo:[1,0,0] neg_hi:[1,0,0]\n\t"
      "v_pk_fma_f32 v[50:51], v[150:151], v[150:151], v[34:35] neg_lo:[1,0,0] neg_hi:[1,0,0]\n\t"
      "v_pk_mul_f32 v[44:45], v[44:45], v[144:145]\n\t"
      "v_pk_mul_f32 v[46:47], v[46:47], v[146:147]\n\t"
      "v_pk_mul_f32 v[48:49], v[48:49], v[148:149]\n\t"
      "v_pk_mul_f32 v[50:51], v[50:51], v[150:151]\n\t"
      "v_exp_f32 v44, v44\n\t"
      "v_exp_f32 v45, v45\n\t"
      "v_exp_f32 v46, v46\n\t"
      "v_exp_f32 v47, v47\n\t"
      "v_exp_f32 v48, v48\n\t"
      "v_exp_f32 v49, v49\n\t"
      "v_exp_f32 v50, v50\n\t"
      "v_exp_f32 v51, v51\n\t"
      "v_pk_fma_f32 v[44:45], v[44:45], v[36:37], v[36:37]\n\t"
      "v_pk_fma_f32 v[46:47], v[46:47], v[36:37], v[36:37]\n\t"
      "v_pk_fma_f32 v[48:49], v[48:49], v[36:37], v[36:37]\n\t"
      "v_pk_fma_f32 v[50:51], v[50:51], v[36:37], v[36:37]\n\t"
      "v_rcp_f32 v44, v44\n\t"
      "v_rcp_f32 v45, v45\n\t"
      "v_rcp_f32 v46, v46\n\t"
      "v_rcp_f32 v47, v47\n\t"
      "v_rcp_f32 v48, v48\n\t"
      "v_rcp_f32 v49, v49\n\t"
      "v_rcp_f32 v50, v50\n\t"
      "v_rcp_f32 v51, v51\n\t"
      "v_pk_mul_f32 v[44:45], v[44:45], v[144:145]\n\t"
      "v_pk_mul_f32 v[46:47], v[46:47], v[146:147]\n\t"
      "v_pk_mul_f32 v[48:49], v[48:49], v[148:149]\n\t"
      "v_pk_mul_f32 v[50:51], v[50:51], v[150:151]\n\t"
      "v_cvt_pk_bf16_f32 v56, v44, v45\n\t"
      "v_cvt_pk_bf16_f32 v57, v46, v47\n\t"
      "v_cvt_pk_bf16_f32 v58, v48, v49\n\t"
      "v_cvt_pk_bf16_f32 v59, v50, v51\n\t"
      "buffer_store_dwordx4 v[56:59], v15, s[24:27], 0 offen offset:1024 nt\n\t"
      "v_pk_fma_f32 v[160:161], v[160:161], v[80:81], v[88:89]\n\t"
      "v_pk_fma_f32 v[162:163], v[162:163], v[82:83], v[90:91]\n\t"
      "v_pk_fma_f32 v[164:165], v[164:165], v[84:85], v[92:93]\n\t"
      "v_pk_fma_f32 v[166:167], v[166:167], v[86:87], v[94:95]\n\t"
      "v_pk_fma_f32 v[44:45], v[160:161], v[160:161], v[34:35] neg_lo:[1,0,0] neg_hi:[1,0,0]\n\t"
      "v_pk_fma_f32 v[46:47], v[162:163], v[162:163], v[34:35] neg_lo:[1,0,0] neg_hi:[1,0,0]\n\t"
      "v_pk_fma_f32 v[48:49], v[164:165], v[164:165], v[34:35] neg_lo:[1,0,0] neg_hi:[1,0,0]\n\t"
      "v_pk_fma_f32 v[50:51], v[166:167], v[166:167], v[34:35] neg_lo:[1,0,0] neg_hi:[1,0,0]\n\t"
      "v_pk_mul_f32 v[44:45], v[44:45], v[160:161]\n\t"
      "v_pk_mul_f32 v[46:47], v[46:47], v[162:163]\n\t"
      "v_pk_mul_f32 v[48:49], v[48:49], v[164:165]\n\t"
      "v_pk_mul_f32 v[50:51], v[50:51], v[166:167]\n\t"
      "v_exp_f32 v44, v44\n\t"
      "v_exp_f32 v45, v45\n\t"
      "v_exp_f32 v46, v46\n\t"
      "v_exp_f32 v47, v47\n\t"
      "v_exp_f32 v48, v48\n\t"
      "v_exp_f32 v49, v49\n\t"
      "v_exp_f32 v50, v50\n\t"
      "v_exp_f32 v51, v51\n\t"
      "v_pk_fma_f32 v[44:45], v[44:45], v[36:37], v[36:37]\n\t"
      "v_pk_fma_f32 v[46:47], v[46:47], v[36:37], v[36:37]\n\t"
      "v_pk_fma_f32 v[48:49], v[48:49], v[36:37], v[36:37]\n\t"
      "v_pk_fma_f32 v[50:51], v[50:51], v[36:37], v[36:37]\n\t"
      "v_rcp_f32 v44, v44\n\t"
      "v_rcp_f32 v45, v45\n\t"
      "v_rcp_f32 v46, v46\n\t"
      "v_rcp_f32 v47, v47\n\t"
      "v_rcp_f32 v48, v48\n\t"
      "v_rcp_f32 v49, v49\n\t"
      "v_rcp_f32 v50, v50\n\t"
      "v_rcp_f32 v51, v51\n\t"
      "v_pk_mul_f32 v[44:45], v[44:45], v[160:161]\n\t"
      "v_pk_mul_f32 v[46:47], v[46:47], v[162:163]\n\t"
      "v_pk_mul_f32 v[48:49], v[48:49], v[164:165]\n\t"
      "v_pk_mul_f32 v[50:51], v[50:51], v[166:167]\n\t"
      "v_cvt_pk_bf16_f32 v52, v44, v45\n\t"
      "v_cvt_pk_bf16_f32 v53, v46, v47\n\t"
      "v_cvt_pk_bf16_f32 v54, v48, v49\n\t"
      "v_cvt_pk_bf16_f32 v55, v50, v51\n\t"
      "buffer_store_dwordx4 v[52:55], v15, s[24:27], 0 offen offset:2048 nt\n\t"
      "v_pk_fma_f32 v[176:177], v[176:177], v[80:81], v[88:89]\n\t"
      "v_pk_fma_f32 v[178:179], v[178:179], v[82:83], v[90:91]\n\t"
      "v_pk_fma_f32 v[180:181], v[180:181], v[84:85], v[92:93]\n\t"
      "v_pk_fma_f32 v[182:183], v[182:183], v[86:87], v[94:95]\n\t"
      "v_pk_fma_f32 v[44:45], v[176:177], v[176:177], v[34:35] neg_lo:[1,0,0] neg_hi:[1,0,0]\n\t"
      "v_pk_fma_f32 v[46:47], v[178:179], v[178:179], v[34:35] neg_lo:[1,0,0] neg_hi:[1,0,0]\n\t"
      "v_pk_fma_f32 v[48:49], v[180:181], v[180:181], v[34:35] neg_lo:[1,0,0] neg_hi:[1,0,0]\n\t"
      "v_pk_fma_f32 v[50:51], v[182:183], v[182:183], v[34:35] neg_lo:[1,0,0] neg_hi:[1,0,0]\n\t"
      "v_pk_mul_f32 v[44:45], v[44:45], v[176:177]\n\t"
      "v_pk_mul_f32 v[46:47], v[46:47], v[178:179]\n\t"
      "v_pk_mul_f32 v[48:49], v[48:49], v[180:181]\n\t"
      "v_pk_mul_f32 v[50:51], v[50:51], v[182:183]\n\t"
      "v_exp_f32 v44, v44\n\t"
      "v_exp_f32 v45, v45\n\t"
      "v_exp_f32 v46, v46\n\t"
      "v_exp_f32 v47, v47\n\t"
      "v_exp_f32 v48, v48\n\t"
      "v_exp_f32 v49, v49\n\t"
      "v_exp_f32 v50, v50\n\t"
      "v_exp_f32 v51, v51\n\t"
      "v_pk_fma_f32 v[44:45], v[44:45], v[36:37], v[36:37]\n\t"
      "v_pk_fma_f32 v[46:47], v[46:47], v[36:37], v[36:37]\n\t"
      "v_pk_fma_f32 v[48:49], v[48:49], v[36:37], v[36:37]\n\t"
      "v_pk_fma_f32 v[50:51], v[50:51], v[36:37], v[36:37]\n\t"
      "v_rcp_f32 v44, v44\n\t"
      "v_rcp_f32 v45, v45\n\t"
      "v_rcp_f32 v46, v46\n\t"
      "v_rcp_f32 v47, v47\n\t"
      "v_rcp_f32 v48, v48\n\t"
      "v_rcp_f32 v49, v49\n\t"
      "v_rcp_f32 v50, v50\n\t"
      "v_rcp_f32 v51, v51\n\t"
      "v_pk_mul_f32 v[44:45], v[44:45], v[176:177]\n\t"
      "v_pk_mul_f32 v[46:47], v[46:47], v[178:179]\n\t"
      "v_pk_mul_f32 v[48:49], v[48:49], v[180:181]\n\t"
      "v_pk_mul_f32 v[50:51], v[50:51], v[182:183]\n\t"
      "v_cvt_pk_bf16_f32 v56, v44, v45\n\t"
      "v_cvt_pk_bf16_f32 v57, v46, v47\n\t"
      "v_cvt_pk_bf16_f32 v58, v48, v49\n\t"
      "v_cvt_pk_bf16_f32 v59, v50, v51\n\t"
      "buffer_store_dwordx4 v[56:59], v15, s[24:27], 0 offen offset:3072 nt\n\t"
      "v_pk_fma_f32 v[192:193], v[192:193], v[80:81], v[88:89]\n\t"
      "v_pk_fma_f32 v[194:195], v[194:195], v[82:83], v[90:91]\n\t"
      "v_pk_fma_f32 v[196:197], v[196:197], v[84:85], v[92:93]\n\t"
      "v_pk_fma_f32 v[198:199], v[198:199], v[86:87], v[94:95]\n\t"
      "v_pk_fma_f32 v[44:45], v[192:193], v[192:193], v[34:35] neg_lo:[1,0,0] neg_hi:[1,0,0]\n\t"
      "v_pk_fma_f32 v[46:47], v[194:195], v[194:195], v[34:35] neg_lo:[1,0,0] neg_hi:[1,0,0]\n\t"
      "v_pk_fma_f32 v[48:49], v[196:197], v[196:197], v[34:35] neg_lo:[1,0,0] neg_hi:[1,0,0]\n\t"
      "v_pk_fma_f32 v[50:51], v[198:199], v[198:199], v[34:35] neg_lo:[1,0,0] neg_hi:[1,0,0]\n\t"
      "v_pk_mul_f32 v[44:45], v[44:45], v[192:193]\n\t"
      "v_pk_mul_f32 v[46:47], v[46:47], v[194:195]\n\t"
      "v_pk_mul_f32 v[48:49], v[48:49], v[196:197]\n\t"
      "v_pk_mul_f32 v[50:51], v[50:51], v[198:199]\n\t"
      "v_exp_f32 v44, v44\n\t"
      "v_exp_f32 v45, v45\n\t"
      "v_exp_f32 v46, v46\n\t"
      "v_exp_f32 v47, v47\n\t"
      "v_exp_f32 v48, v48\n\t"
      "v_exp_f32 v49, v49\n\t"
      "v_exp_f32 v50, v50\n\t"
      "v_exp_f32 v51, v51\n\t"
      "v_pk_fma_f32 v[44:45], v[44:45], v[36:37], v[36:37]\n\t"
      "v_pk_fma_f32 v[46:47], v[46:47], v[36:37], v[36:37]\n\t"
      "v_pk_fma_f32 v[48:49], v[48:49], v[36:37], v[36:37]\n\t"
      "v_pk_fma_f32 v[50:51], v[50:51], v[36:37], v[36:37]\n\t"
      "v_rcp_f32 v44, v44\n\t"
      "v_rcp_f32 v45, v45\n\t"
      "v_rcp_f32 v46, v46\n\t"
      "v_rcp_f32 v47, v47\n\t"
      "v_rcp_f32 v48, v48\n\t"
      "v_rcp_f32 v49, v49\n\t"
      "v_rcp_f32 v50, v50\n\t"
      "v_rcp_f32 v51, v51\n\t"
      "v_pk_mul_f32 v[44:45], v[44:45], v[192:193]\n\t"
      "v_pk_mul_f32 v[46:47], v[46:47], v[194:195]\n\t"
      "v_pk_mul_f32 v[48:49], v[48:49], v[196:197]\n\t"
      "v_pk_mul_f32 v[50:51], v[50:51], v[198:199]\n\t"
      "v_cvt_pk_bf16_f32 v52, v44, v45\n\t"
      "v_cvt_pk_bf16_f32 v53, v46, v47\n\t"
      "v_cvt_pk_bf16_f32 v54, v48, v49\n\t"
      "v_cvt_pk_bf16_f32 v55, v50, v51\n\t"
      "buffer_store_dwordx4 v[52:55], v15, s[24:27], s28 offen offset:0 nt\n\t"
      "v_pk_fma_f32 v[208:209], v[208:209], v[80:81], v[88:89]\n\t"
      "v_pk_fma_f32 v[210:211], v[210:211], v[82:83], v[90:91]\n\t"
      "v_pk_fma_f32 v[212:213], v[212:213], v[84:85], v[92:93]\n\t"
      "v_pk_fma_f32 v[214:215], v[214:215], v[86:87], v[94:95]\n\t"
      "v_pk_fma_f32 v[44:45], v[208:209], v[208:209], v[34:35] neg_lo:[1,0,0] neg_hi:[1,0,0]\n\t"
      "v_pk_fma_f32 v[46:47], v[210:211], v[210:211], v[34:35] neg_lo:[1,0,0] neg_hi:[1,0,0]\n\t"
      "v_pk_fma_f32 v[48:49], v[212:213], v[212:213], v[34:35] neg_lo:[1,0,0] neg_hi:[1,0,0]\n\t"
      "v_pk_fma_f32 v[50:51], v[214:215], v[214:215], v[34:35] neg_lo:[1,0,0] neg_hi:[1,0,0]\n\t"
      "v_pk_mul_f32 v[44:45], v[44:45], v[208:209]\n\t"
      "v_pk_mul_f32 v[46:47], v[46:47], v[210:211]\n\t"
      "v_pk_mul_f32 v[48:49], v[48:49], v[212:213]\n\t"
      "v_pk_mul_f32 v[50:51], v[50:51], v[214:215]\n\t"
      "v_exp_f32 v44, v44\n\t"
      "v_exp_f32 v45, v45\n\t"
      "v_exp_f32 v46, v46\n\t"
      "v_exp_f32 v47, v47\n\t"
      "v_exp_f32 v48, v48\n\t"
      "v_exp_f32 v49, v49\n\t"
      "v_exp_f32 v50, v50\n\t"
      "v_exp_f32 v51, v51\n\t"
      "v_pk_fma_f32 v[44:45], v[44:45], v[36:37], v[36:37]\n\t"
      "v_pk_fma_f32 v[46:47], v[46:47], v[36:37], v[36:37]\n\t"
      "v_pk_fma_f32 v[48:49], v[48:49], v[36:37], v[36:37]\n\t"
      "v_pk_fma_f32 v[50:51], v[50:51], v[36:37], v[36:37]\n\t"
      "v_rcp_f32 v44, v44\n\t"
      "v_rcp_f32 v45, v45\n\t"
      "v_rcp_f32 v46, v46\n\t"
      "v_rcp_f32 v47, v47\n\t"
      "v_rcp_f32 v48, v48\n\t"
      "v_rcp_f32 v49, v49\n\t"
      "v_rcp_f32 v50, v50\n\t"
      "v_rcp_f32 v51, v51\n\t"
      "v_pk_mul_f32 v[44:45], v[44:45], v[208:209]\n\t"
      "v_pk_mul_f32 v[46:47], v[46:47], v[210:211]\n\t"
      "v_pk_mul_f32 v[48:49], v[48:49], v[212:213]\n\t"
      "v_pk_mul_f32 v[50:51], v[50:51], v[214:215]\n\t"
      "v_cvt_pk_bf16_f32 v56, v44, v45\n\t"
      "v_cvt_pk_bf16_f32 v57, v46, v47\n\t"
      "v_cvt_pk_bf16_f32 v58, v48, v49\n\t"
      "v_cvt_pk_bf16_f32 v59, v50, v51\n\t"
      "buffer_store_dwordx4 v[56:59], v15, s[24:27], s28 offen offset:1024 nt\n\t"
      "v_pk_fma_f32 v[224:225], v[224:225], v[80:81], v[88:89]\n\t"
      "v_pk_fma_f32 v[226:227], v[226:227], v[82:83], v[90:91]\n\t"
      "v_pk_fma_f32 v[228:229], v[228:229], v[84:85], v[92:93]\n\t"
      "v_pk_fma_f32 v[230:231], v[230:231], v[86:87], v[94:95]\n\t"
      "v_pk_fma_f32 v[44:45], v[224:225], v[224:225], v[34:35] neg_lo:[1,0,0] neg_hi:[1,0,0]\n\t"
      "v_pk_fma_f32 v[46:47], v[226:227], v[226:227], v[34:35] neg_lo:[1,0,0] neg_hi:[1,0,0]\n\t"
      "v_pk_fma_f32 v[48:49], v[228:229], v[228:229], v[34:35] neg_lo:[1,0,0] neg_hi:[1,0,0]\n\t"
      "v_pk_fma_f32 v[50:51], v[230:231], v[230:231], v[34:35] neg_lo:[1,0,0] neg_hi:[1,0,0]\n\t"
      "v_pk_mul_f32 v[44:45], v[44:45], v[224:225]\n\t"
      "v_pk_mul_f32 v[46:47], v[46:47], v[226:227]\n\t"
      "v_pk_mul_f32 v[48:49], v[48:49], v[228:229]\n\t"
      "v_pk_mul_f32 v[50:51], v[50:51], v[230:231]\n\t"
      "v_exp_f32 v44, v44\n\t"
      "v_exp_f32 v45, v45\n\t"
      "v_exp_f32 v46, v46\n\t"
      "v_exp_f32 v47, v47\n\t"
      "v_exp_f32 v48, v48\n\t"
      "v_exp_f32 v49, v49\n\t"
      "v_exp_f32 v50, v50\n\t"
      "v_exp_f32 v51, v51\n\t"
      "v_pk_fma_f32 v[44:45], v[44:45], v[36:37], v[36:37]\n\t"
      "v_pk_fma_f32 v[46:47], v[46:47], v[36:37], v[36:37]\n\t"
      "v_pk_fma_f32 v[48:49], v[48:49], v[36:37], v[36:37]\n\t"
      "v_pk_fma_f32 v[50:51], v[50:51], v[36:37], v[36:37]\n\t"
      "v_rcp_f32 v44, v44\n\t"
      "v_rcp_f32 v45, v45\n\t"
      "v_rcp_f32 v46, v46\n\t"
      "v_rcp_f32 v47, v47\n\t"
      "v_rcp_f32 v48, v48\n\t"
      "v_rcp_f32 v49, v49\n\t"
      "v_rcp_f32 v50, v50\n\t"
      "v_rcp_f32 v51, v51\n\t"
      "v_pk_mul_f32 v[44:45], v[44:45], v[224:225]\n\t"
      "v_pk_mul_f32 v[46:47], v[46:47], v[226:227]\n\t"
      "v_pk_mul_f32 v[48:49], v[48:49], v[228:229]\n\t"
      "v_pk_mul_f32 v[50:51], v[50:51], v[230:231]\n\t"
      "v_cvt_pk_bf16_f32 v52, v44, v45\n\t"
      "v_cvt_pk_bf16_f32 v53, v46, v47\n\t"
      "v_cvt_pk_bf16_f32 v54, v48, v49\n\t"
      "v_cvt_pk_bf16_f32 v55, v50, v51\n\t"
      "buffer_store_dwordx4 v[52:55], v15, s[24:27], s28 offen offset:2048 nt\n\t"
      "ds_read_b32 v117, v7 offset:8\n\t"
      "ds_read_b128 v[96:99], v14 offset:128\n\t"
      "ds_read_b128 v[100:103], v14 offset:144\n\t"
      "ds_read_b128 v[104:107], v14 offset:640\n\t"
      "ds_read_b128 v[108:111], v14 offset:656\n\t"
      "v_pk_fma_f32 v[240:241], v[240:241], v[80:81], v[88:89]\n\t"
      "v_pk_fma_f32 v[242:243], v[242:243], v[82:83], v[90:91]\n\t"
      "v_pk_fma_f32 v[244:245], v[244:245], v[84:85], v[92:93]\n\t"
      "v_pk_fma_f32 v[246:247], v[246:247], v[86:87], v[94:95]\n\t"
      "v_pk_fma_f32 v[44:45], v[240:241], v[240:241], v[34:35] neg_lo:[1,0,0] neg_hi:[1,0,0]\n\t"
      "v_pk_fma_f32 v[46:47], v[242:243], v[242:243], v[34:35] neg_lo:[1,0,0] neg_hi:[1,0,0]\n\t"
      "v_pk_fma_f32 v[48:49], v[244:245], v[244:245], v[34:35] neg_lo:[1,0,0] neg_hi:[1,0,0]\n\t"
      "v_pk_fma_f32 v[50:51], v[246:247], v[246:247], v[34:35] neg_lo:[1,0,0] neg_hi:[1,0,0]\n\t"
      "v_pk_mul_f32 v[44:45], v[44:45], v[240:241]\n\t"
      "v_pk_mul_f32 v[46:47], v[46:47], v[242:243]\n\t"
      "v_pk_mul_f32 v[48:49], v[48:49], v[244:245]\n\t"
      "v_pk_mul_f32 v[50:51], v[50:51], v[246:247]\n\t"
      "v_exp_f32 v44, v44\n\t"
      "v_exp_f32 v45, v45\n\t"
      "v_exp_f32 v46, v46\n\t"
      "v_exp_f32 v47, v47\n\t"
      "v_exp_f32 v48, v48\n\t"
      "v_exp_f32 v49, v49\n\t"
      "v_exp_f32 v50, v50\n\t"
      "v_exp_f32 v51, v51\n\t"
      "v_pk_fma_f32 v[44:45], v[44:45], v[36:37], v[36:37]\n\t"
      "v_pk_fma_f32 v[46:47], v[46:47], v[36:37], v[36:37]\n\t"
      "v_pk_fma_f32 v[48:49], v[48:49], v[36:37], v[36:37]\n\t"
      "v_pk_fma_f32 v[50:51], v[50:51], v[36:37], v[36:37]\n\t"
      "v_rcp_f32 v44, v44\n\t"
      "v_rcp_f32 v45, v45\n\t"
      "v_rcp_f32 v46, v46\n\t"
      "v_rcp_f32 v47, v47\n\t"
      "v_rcp_f32 v48, v48\n\t"
      "v_rcp_f32 v49, v49\n\t"
      "v_rcp_f32 v50, v50\n\t"
      "v_rcp_f32 v51, v51\n\t"
      "v_pk_mul_f32 v[44:45], v[44:45], v[240:241]\n\t"
      "v_pk_mul_f32 v[46:47], v[46:47], v[242:243]\n\t"
      "v_pk_mul_f32 v[48:49], v[48:49], v[244:245]\n\t"
      "v_pk_mul_f32 v[50:51], v[50:51], v[246:247]\n\t"
      "v_cvt_pk_bf16_f32 v56, v44, v45\n\t"
      "v_cvt_pk_bf16_f32 v57, v46, v47\n\t"
      "v_cvt_pk_bf16_f32 v58, v48, v49\n\t"
      "v_cvt_pk_bf16_f32 v59, v50, v51\n\t"
      "buffer_store_dwordx4 v[56:59], v15, s[24:27], s28 offen offset:3072 nt\n\t"
      "s_waitcnt lgkmcnt(0)\n\t"
      "v_cmp_eq_u32 vcc, 0, v117\n\t"
      "s_cbranch_vccz conv_go1\n\t"
      "conv_wait1:\n\t"
      "ds_read_b32 v117, v7 offset:8\n\t"
      "ds_read_b128 v[96:99], v14 offset:128\n\t"
      "ds_read_b128 v[100:103], v14 offset:144\n\t"
      "ds_read_b128 v[104:107], v14 offset:640\n\t"
      "ds_read_b128 v[108:111], v14 offset:656\n\t"
      "s_waitcnt lgkmcnt(4)\n\t"
      "v_cmp_eq_u32 vcc, 0, v117\n\t"
      "s_waitcnt lgkmcnt(0)\n\t"
      "s_cbranch_vccnz conv_wait1\n\t"
      "conv_go1:\n\t"
      "v_pk_fma_f32 v[104:105], v[26:27], v[96:97], v[104:105]\n\t"
      "v_pk_fma_f32 v[106:107], v[28:29], v[98:99], v[106:107]\n\t"
      "v_pk_fma_f32 v[108:109], v[30:31], v[100:101], v[108:109]\n\t"
      "v_pk_fma_f32 v[110:111], v[32:33], v[102:103], v[110:111]\n\t"
      "v_pk_fma_f32 v[136:137], v[136:137], v[96:97], v[104:105]\n\t"
      "v_pk_fma_f32 v[138:139], v[138:139], v[98:99], v[106:107]\n\t"
      "v_pk_fma_f32 v[140:141], v[140:141], v[100:101], v[108:109]\n\t"
      "v_pk_fma_f32 v[142:143], v[142:143], v[102:103], v[110:111]\n\t"
      "v_pk_fma_f32 v[44:45], v[136:137], v[136:137], v[34:35] neg_lo:[1,0,0] neg_hi:[1,0,0]\n\t"
      "v_pk_fma_f32 v[46:47], v[138:139], v[138:139], v[34:35] neg_lo:[1,0,0] neg_hi:[1,0,0]\n\t"
      "v_pk_fma_f32 v[48:49], v[140:141], v[140:141], v[34:35] neg_lo:[1,0,0] neg_hi:[1,0,0]\n\t"
      "v_pk_fma_f32 v[50:51], v[142:143], v[142:143], v[34:35] neg_lo:[1,0,0] neg_hi:[1,0,0]\n\t"
      "v_pk_mul_f32 v[44:45], v[44:45], v[136:137]\n\t"
      "v_pk_mul_f32 v[46:47], v[46:47], v[138:139]\n\t"
      "v_pk_mul_f32 v[48:49], v[48:49], v[140:141]\n\t"
      "v_pk_mul_f32 v[50:51], v[50:51], v[142:143]\n\t"
      "v_exp_f32 v44, v44\n\t"
      "v_exp_f32 v45, v45\n\t"
      "v_exp_f32 v46, v46\n\t"
      "v_exp_f32 v47, v47\n\t"
      "v_exp_f32 v48, v48\n\t"
      "v_exp_f32 v49, v49\n\t"
      "v_exp_f32 v50, v50\n\t"
      "v_exp_f32 v51, v51\n\t"
      "v_pk_fma_f32 v[44:45], v[44:45], v[36:37], v[36:37]\n\t"
      "v_pk_fma_f32 v[46:47], v[46:47], v[36:37], v[36:37]\n\t"
      "v_pk_fma_f32 v[48:49], v[48:49], v[36:37], v[36:37]\n\t"
      "v_pk_fma_f32 v[50:51], v[50:51], v[36:37], v[36:37]\n\t"
      "v_rcp_f32 v44, v44\n\t"
      "v_rcp_f32 v45, v45\n\t"
      "v_rcp_f32 v46, v46\n\t"
      "v_rcp_f32 v47, v47\n\t"
      "v_rcp_f32 v48, v48\n\t"
      "v_rcp_f32 v49, v49\n\t"
      "v_rcp_f32 v50, v50\n\t"
      "v_rcp_f32 v51, v51\n\t"
      "v_pk_mul_f32 v[44:45], v[44:45], v[136:137]\n\t"
      "v_pk_mul_f32 v[46:47], v[46:47], v[138:139]\n\t"
      "v_pk_mul_f32 v[48:49], v[48:49], v[140:141]\n\t"
      "v_pk_mul_f32 v[50:51], v[50:51], v[142:143]\n\t"
      "v_cvt_pk_bf16_f32 v52, v44, v45\n\t"
      "v_cvt_pk_bf16_f32 v53, v46, v47\n\t"
      "v_cvt_pk_bf16_f32 v54, v48, v49\n\t"
      "v_cvt_pk_bf16_f32 v55, v50, v51\n\t"
      "buffer_store_dwordx4 v[52:55], v15, s[24:27], 0 offen offset:64 nt\n\t"
      "v_pk_fma_f32 v[152:153], v[152:153], v[96:97], v[104:105]\n\t"
      "v_pk_fma_f32 v[154:155], v[154:155], v[98:99], v[106:107]\n\t"
      "v_pk_fma_f32 v[156:157], v[156:157], v[100:101], v[108:109]\n\t"
      "v_pk_fma_f32 v[158:159], v[158:159], v[102:103], v[110:111]\n\t"
      "v_pk_fma_f32 v[44:45], v[152:153], v[152:153], v[34:35] neg_lo:[1,0,0] neg_hi:[1,0,0]\n\t"
      "v_pk_fma_f32 v[46:47], v[154:155], v[154:155], v[34:35] neg_lo:[1,0,0] neg_hi:[1,0,0]\n\t"
      "v_pk_fma_f32 v[48:49], v[156:157], v[156:157], v[34:35] neg_lo:[1,0,0] neg_hi:[1,0,0]\n\t"
      "v_pk_fma_f32 v[50:51], v[158:159], v[158:159], v[34:35] neg_lo:[1,0,0] neg_hi:[1,0,0]\n\t"
      "v_pk_mul_f32 v[44:45], v[44:45], v[152:153]\n\t"
      "v_pk_mul_f32 v[46:47], v[46:47], v[154:155]\n\t"
      "v_pk_mul_f32 v[48:49], v[48:49], v[156:157]\n\t"
      "v_pk_mul_f32 v[50:51], v[50:51], v[158:159]\n\t"
      "v_exp_f32 v44, v44\n\t"
      "v_exp_f32 v45, v45\n\t"
      "v_exp_f32 v46, v46\n\t"
      "v_exp_f32 v47, v47\n\t"
      "v_exp_f32 v48, v48\n\t"
      "v_exp_f32 v49, v49\n\t"
      "v_exp_f32 v50, v50\n\t"
      "v_exp_f32 v51, v51\n\t"
      "v_pk_fma_f32 v[44:45], v[44:45], v[36:37], v[36:37]\n\t"
      "v_pk_fma_f32 v[46:47], v[46:47], v[36:37], v[36:37]\n\t"
      "v_pk_fma_f32 v[48:49], v[48:49], v[36:37], v[36:37]\n\t"
      "v_pk_fma_f32 v[50:51], v[50:51], v[36:37], v[36:37]\n\t"
      "v_rcp_f32 v44, v44\n\t"
      "v_rcp_f32 v45, v45\n\t"
      "v_rcp_f32 v46, v46\n\t"
      "v_rcp_f32 v47, v47\n\t"
      "v_rcp_f32 v48, v48\n\t"
      "v_rcp_f32 v49, v49\n\t"
      "v_rcp_f32 v50, v50\n\t"
      "v_rcp_f32 v51, v51\n\t"
      "v_pk_mul_f32 v[44:45], v[44:45], v[152:153]\n\t"
      "v_pk_mul_f32 v[46:47], v[46:47], v[154:155]\n\t"
      "v_pk_mul_f32 v[48:49], v[48:49], v[156:157]\n\t"
      "v_pk_mul_f32 v[50:51], v[50:51], v[158:159]\n\t"
      "v_cvt_pk_bf16_f32 v56, v44, v45\n\t"
      "v_cvt_pk_bf16_f32 v57, v46, v47\n\t"
      "v_cvt_pk_bf16_f32 v58, v48, v49\n\t"
      "v_cvt_pk_bf16_f32 v59, v50, v51\n\t"
      "buffer_store_dwordx4 v[56:59], v15, s[24:27], 0 offen offset:1088 nt\n\t"
      "v_pk_fma_f32 v[168:169], v[168:169], v[96:97], v[104:105]\n\t"
      "v_pk_fma_f32 v[170:171], v[170:171], v[98:99], v[106:107]\n\t"
      "v_pk_fma_f32 v[172:173], v[172:173], v[100:101], v[108:109]\n\t"
      "v_pk_fma_f32 v[174:175], v[174:175], v[102:103], v[110:111]\n\t"
      "v_pk_fma_f32 v[44:45], v[168:169], v[168:169], v[34:35] neg_lo:[1,0,0] neg_hi:[1,0,0]\n\t"
      "v_pk_fma_f32 v[46:47], v[170:171], v[170:171], v[34:35] neg_lo:[1,0,0] neg_hi:[1,0,0]\n\t"
      "v_pk_fma_f32 v[48:49], v[172:173], v[172:173], v[34:35] neg_lo:[1,0,0] neg_hi:[1,0,0]\n\t"
      "v_pk_fma_f32 v[50:51], v[174:175], v[174:175], v[34:35] neg_lo:[1,0,0] neg_hi:[1,0,0]\n\t"
      "v_pk_mul_f32 v[44:45], v[44:45], v[168:169]\n\t"
      "v_pk_mul_f32 v[46:47], v[46:47], v[170:171]\n\t"
      "v_pk_mul_f32 v[48:49], v[48:49], v[172:173]\n\t"
      "v_pk_mul_f32 v[50:51], v[50:51], v[174:175]\n\t"
      "v_exp_f32 v44, v44\n\t"
      "v_exp_f32 v45, v45\n\t"
      "v_exp_f32 v46, v46\n\t"
      "v_exp_f32 v47, v47\n\t"
      "v_exp_f32 v48, v48\n\t"
      "v_exp_f32 v49, v49\n\t"
      "v_exp_f32 v50, v50\n\t"
      "v_exp_f32 v51, v51\n\t"
      "v_pk_fma_f32 v[44:45], v[44:45], v[36:37], v[36:37]\n\t"
      "v_pk_fma_f32 v[46:47], v[46:47], v[36:37], v[36:37]\n\t"
      "v_pk_fma_f32 v[48:49], v[48:49], v[36:37], v[36:37]\n\t"
      "v_pk_fma_f32 v[50:51], v[50:51], v[36:37], v[36:37]\n\t"
      "v_rcp_f32 v44, v44\n\t"
      "v_rcp_f32 v45, v45\n\t"
      "v_rcp_f32 v46, v46\n\t"
      "v_rcp_f32 v47, v47\n\t"
      "v_rcp_f32 v48, v48\n\t"
      "v_rcp_f32 v49, v49\n\t"
      "v_rcp_f32 v50, v50\n\t"
      "v_rcp_f32 v51, v51\n\t"
      "v_pk_mul_f32 v[44:45], v[44:45], v[168:169]\n\t"
      "v_pk_mul_f32 v[46:47], v[46:47], v[170:171]\n\t"
      "v_pk_mul_f32 v[48:49], v[48:49], v[172:173]\n\t"
      "v_pk_mul_f32 v[50:51], v[50:51], v[174:175]\n\t"
      "v_cvt_pk_bf16_f32 v52, v44, v45\n\t"
      "v_cvt_pk_bf16_f32 v53, v46, v47\n\t"
      "v_cvt_pk_bf16_f32 v54, v48, v49\n\t"
      "v_cvt_pk_bf16_f32 v55, v50, v51\n\t"
      "buffer_store_dwordx4 v[52:55], v15, s[24:27], 0 offen offset:2112 nt\n\t"
      "v_pk_fma_f32 v[184:185], v[184:185], v[96:97], v[104:105]\n\t"
      "v_pk_fma_f32 v[186:187], v[186:187], v[98:99], v[106:107]\n\t"
      "v_pk_fma_f32 v[188:189], v[188:189], v[100:101], v[108:109]\n\t"
      "v_pk_fma_f32 v[190:191], v[190:191], v[102:103], v[110:111]\n\t"
      "v_pk_fma_f32 v[44:45], v[184:185], v[184:185], v[34:35] neg_lo:[1,0,0] neg_hi:[1,0,0]\n\t"
      "v_pk_fma_f32 v[46:47], v[186:187], v[186:187], v[34:35] neg_lo:[1,0,0] neg_hi:[1,0,0]\n\t"
      "v_pk_fma_f32 v[48:49], v[188:189], v[188:189], v[34:35] neg_lo:[1,0,0] neg_hi:[1,0,0]\n\t"
      "v_pk_fma_f32 v[50:51], v[190:191], v[190:191], v[34:35] neg_lo:[1,0,0] neg_hi:[1,0,0]\n\t"
      "v_pk_mul_f32 v[44:45], v[44:45], v[184:185]\n\t"
      "v_pk_mul_f32 v[46:47], v[46:47], v[186:187]\n\t"
      "v_pk_mul_f32 v[48:49], v[48:49], v[188:189]\n\t"
      "v_pk_mul_f32 v[50:51], v[50:51], v[190:191]\n\t"
      "v_exp_f32 v44, v44\n\t"
      "v_exp_f32 v45, v45\n\t"
      "v_exp_f32 v46, v46\n\t"
      "v_exp_f32 v47, v47\n\t"
      "v_exp_f32 v48, v48\n\t"
      "v_exp_f32 v49, v49\n\t"
      "v_exp_f32 v50, v50\n\t"
      "v_exp_f32 v51, v51\n\t"
      "v_pk_fma_f32 v[44:45], v[44:45], v[36:37], v[36:37]\n\t"
      "v_pk_fma_f32 v[46:47], v[46:47], v[36:37], v[36:37]\n\t"
      "v_pk_fma_f32 v[48:49], v[48:49], v[36:37], v[36:37]\n\t"
      "v_pk_fma_f32 v[50:51], v[50:51], v[36:37], v[36:37]\n\t"
      "v_rcp_f32 v44, v44\n\t"
      "v_rcp_f32 v45, v45\n\t"
      "v_rcp_f32 v46, v46\n\t"
      "v_rcp_f32 v47, v47\n\t"
      "v_rcp_f32 v48, v48\n\t"
      "v_rcp_f32 v49, v49\n\t"
      "v_rcp_f32 v50, v50\n\t"
      "v_rcp_f32 v51, v51\n\t"
      "v_pk_mul_f32 v[44:45], v[44:45], v[184:185]\n\t"
      "v_pk_mul_f32 v[46:47], v[46:47], v[186:187]\n\t"
      "v_pk_mul_f32 v[48:49], v[48:49], v[188:189]\n\t"
      "v_pk_mul_f32 v[50:51], v[50:51], v[190:191]\n\t"
      "v_cvt_pk_bf16_f32 v56, v44, v45\n\t"
      "v_cvt_pk_bf16_f32 v57, v46, v47\n\t"
      "v_cvt_pk_bf16_f32 v58, v48, v49\n\t"
      "v_cvt_pk_bf16_f32 v59, v50, v51\n\t"
      "buffer_store_dwordx4 v[56:59], v15, s[24:27], 0 offen offset:3136 nt\n\t"
      "v_pk_fma_f32 v[200:201], v[200:201], v[96:97], v[104:105]\n\t"
      "v_pk_fma_f32 v[202:203], v[202:203], v[98:99], v[106:107]\n\t"
      "v_pk_fma_f32 v[204:205], v[204:205], v[100:101], v[108:109]\n\t"
      "v_pk_fma_f32 v[206:207], v[206:207], v[102:103], v[110:111]\n\t"
      "v_pk_fma_f32 v[44:45], v[200:201], v[200:201], v[34:35] neg_lo:[1,0,0] neg_hi:[1,0,0]\n\t"
      "v_pk_fma_f32 v[46:47], v[202:203], v[202:203], v[34:35] neg_lo:[1,0,0] neg_hi:[1,0,0]\n\t"
      "v_pk_fma_f32 v[48:49], v[204:205], v[204:205], v[34:35] neg_lo:[1,0,0] neg_hi:[1,0,0]\n\t"
      "v_pk_fma_f32 v[50:51], v[206:207], v[206:207], v[34:35] neg_lo:[1,0,0] neg_hi:[1,0,0]\n\t"
      "v_pk_mul_f32 v[44:45], v[44:45], v[200:201]\n\t"
      "v_pk_mul_f32 v[46:47], v[46:47], v[202:203]\n\t"
      "v_pk_mul_f32 v[48:49], v[48:49], v[204:205]\n\t"
      "v_pk_mul_f32 v[50:51], v[50:51], v[206:207]\n\t"
      "v_exp_f32 v44, v44\n\t"
      "v_exp_f32 v45, v45\n\t"
      "v_exp_f32 v46, v46\n\t"
      "v_exp_f32 v47, v47\n\t"
      "v_exp_f32 v48, v48\n\t"
      "v_exp_f32 v49, v49\n\t"
      "v_exp_f32 v50, v50\n\t"
      "v_exp_f32 v51, v51\n\t"
      "v_pk_fma_f32 v[44:45], v[44:45], v[36:37], v[36:37]\n\t"
      "v_pk_fma_f32 v[46:47], v[46:47], v[36:37], v[36:37]\n\t"
      "v_pk_fma_f32 v[48:49], v[48:49], v[36:37], v[36:37]\n\t"
      "v_pk_fma_f32 v[50:51], v[50:51], v[36:37], v[36:37]\n\t"
      "v_rcp_f32 v44, v44\n\t"
      "v_rcp_f32 v45, v45\n\t"
      "v_rcp_f32 v46, v46\n\t"
      "v_rcp_f32 v47, v47\n\t"
      "v_rcp_f32 v48, v48\n\t"
      "v_rcp_f32 v49, v49\n\t"
      "v_rcp_f32 v50, v50\n\t"
      "v_rcp_f32 v51, v51\n\t"
      "v_pk_mul_f32 v[44:45], v[44:45], v[200:201]\n\t"
      "v_pk_mul_f32 v[46:47], v[46:47], v[202:203]\n\t"
      "v_pk_mul_f32 v[48:49], v[48:49], v[204:205]\n\t"
      "v_pk_mul_f32 v[50:51], v[50:51], v[206:207]\n\t"
      "v_cvt_pk_bf16_f32 v52, v44, v45\n\t"
      "v_cvt_pk_bf16_f32 v53, v46, v47\n\t"
      "v_cvt_pk_bf16_f32 v54, v48, v49\n\t"
      "v_cvt_pk_bf16_f32 v55, v50, v51\n\t"
      "buffer_store_dwordx4 v[52:55], v15, s[24:27], s28 offen offset:64 nt\n\t"
      "v_pk_fma_f32 v[216:217], v[216:217], v[96:97], v[104:105]\n\t"
      "v_pk_fma_f32 v[218:219], v[218:219], v[98:99], v[106:107]\n\t"
      "v_pk_fma_f32 v[220:221], v[220:221], v[100:101], v[108:109]\n\t"
      "v_pk_fma_f32 v[222:223], v[222:223], v[102:103], v[110:111]\n\t"
      "v_pk_fma_f32 v[44:45], v[216:217], v[216:217], v[34:35] neg_lo:[1,0,0] neg_hi:[1,0,0]\n\t"
      "v_pk_fma_f32 v[46:47], v[218:219], v[218:219], v[34:35] neg_lo:[1,0,0] neg_hi:[1,0,0]\n\t"
      "v_pk_fma_f32 v[48:49], v[220:221], v[220:221], v[34:35] neg_lo:[1,0,0] neg_hi:[1,0,0]\n\t"
      "v_pk_fma_f32 v[50:51], v[222:223], v[222:223], v[34:35] neg_lo:[1,0,0] neg_hi:[1,0,0]\n\t"
      "v_pk_mul_f32 v[44:45], v[44:45], v[216:217]\n\t"
      "v_pk_mul_f32 v[46:47], v[46:47], v[218:219]\n\t"
      "v_pk_mul_f32 v[48:49], v[48:49], v[220:221]\n\t"
      "v_pk_mul_f32 v[50:51], v[50:51], v[222:223]\n\t"
      "v_exp_f32 v44, v44\n\t"
      "v_exp_f32 v45, v45\n\t"
      "v_exp_f32 v46, v46\n\t"
      "v_exp_f32 v47, v47\n\t"
      "v_exp_f32 v48, v48\n\t"
      "v_exp_f32 v49, v49\n\t"
      "v_exp_f32 v50, v50\n\t"
      "v_exp_f32 v51, v51\n\t"
      "v_pk_fma_f32 v[44:45], v[44:45], v[36:37], v[36:37]\n\t"
      "v_pk_fma_f32 v[46:47], v[46:47], v[36:37], v[36:37]\n\t"
      "v_pk_fma_f32 v[48:49], v[48:49], v[36:37], v[36:37]\n\t"
      "v_pk_fma_f32 v[50:51], v[50:51], v[36:37], v[36:37]\n\t"
      "v_rcp_f32 v44, v44\n\t"
      "v_rcp_f32 v45, v45\n\t"
      "v_rcp_f32 v46, v46\n\t"
      "v_rcp_f32 v47, v47\n\t"
      "v_rcp_f32 v48, v48\n\t"
      "v_rcp_f32 v49, v49\n\t"
      "v_rcp_f32 v50, v50\n\t"
      "v_rcp_f32 v51, v51\n\t"
      "v_pk_mul_f32 v[44:45], v[44:45], v[216:217]\n\t"
      "v_pk_mul_f32 v[46:47], v[46:47], v[218:219]\n\t"
      "v_pk_mul_f32 v[48:49], v[48:49], v[220:221]\n\t"
      "v_pk_mul_f32 v[50:51], v[50:51], v[222:223]\n\t"
      "v_cvt_pk_bf16_f32 v56, v44, v45\n\t"
      "v_cvt_pk_bf16_f32 v57, v46, v47\n\t"
      "v_cvt_pk_bf16_f32 v58, v48, v49\n\t"
      "v_cvt_pk_bf16_f32 v59, v50, v51\n\t"
      "buffer_store_dwordx4 v[56:59], v15, s[24:27], s28 offen offset:1088 nt\n\t"
      "v_pk_fma_f32 v[232:233], v[232:233], v[96:97], v[104:105]\n\t"
      "v_pk_fma_f32 v[234:235], v[234:235], v[98:99], v[106:107]\n\t"
      "v_pk_fma_f32 v[236:237], v[236:237], v[100:101], v[108:109]\n\t"
      "v_pk_fma_f32 v[238:239], v[238:239], v[102:103], v[110:111]\n\t"
      "v_pk_fma_f32 v[44:45], v[232:233], v[232:233], v[34:35] neg_lo:[1,0,0] neg_hi:[1,0,0]\n\t"
      "v_pk_fma_f32 v[46:47], v[234:235], v[234:235], v[34:35] neg_lo:[1,0,0] neg_hi:[1,0,0]\n\t"
      "v_pk_fma_f32 v[48:49], v[236:237], v[236:237], v[34:35] neg_lo:[1,0,0] neg_hi:[1,0,0]\n\t"
      "v_pk_fma_f32 v[50:51], v[238:239], v[238:239], v[34:35] neg_lo:[1,0,0] neg_hi:[1,0,0]\n\t"
      "v_pk_mul_f32 v[44:45], v[44:45], v[232:233]\n\t"
      "v_pk_mul_f32 v[46:47], v[46:47], v[234:235]\n\t"
      "v_pk_mul_f32 v[48:49], v[48:49], v[236:237]\n\t"
      "v_pk_mul_f32 v[50:51], v[50:51], v[238:239]\n\t"
      "v_exp_f32 v44, v44\n\t"
      "v_exp_f32 v45, v45\n\t"
      "v_exp_f32 v46, v46\n\t"
      "v_exp_f32 v47, v47\n\t"
      "v_exp_f32 v48, v48\n\t"
      "v_exp_f32 v49, v49\n\t"
      "v_exp_f32 v50, v50\n\t"
      "v_exp_f32 v51, v51\n\t"
      "v_pk_fma_f32 v[44:45], v[44:45], v[36:37], v[36:37]\n\t"
      "v_pk_fma_f32 v[46:47], v[46:47], v[36:37], v[36:37]\n\t"
      "v_pk_fma_f32 v[48:49], v[48:49], v[36:37], v[36:37]\n\t"
      "v_pk_fma_f32 v[50:51], v[50:51], v[36:37], v[36:37]\n\t"
      "v_rcp_f32 v44, v44\n\t"
      "v_rcp_f32 v45, v45\n\t"
      "v_rcp_f32 v46, v46\n\t"
      "v_rcp_f32 v47, v47\n\t"
      "v_rcp_f32 v48, v48\n\t"
      "v_rcp_f32 v49, v49\n\t"
      "v_rcp_f32 v50, v50\n\t"
      "v_rcp_f32 v51, v51\n\t"
      "v_pk_mul_f32 v[44:45], v[44:45], v[232:233]\n\t"
      "v_pk_mul_f32 v[46:47], v[46:47], v[234:235]\n\t"
      "v_pk_mul_f32 v[48:49], v[48:49], v[236:237]\n\t"
      "v_pk_mul_f32 v[50:51], v[50:51], v[238:239]\n\t"
      "v_cvt_pk_bf16_f32 v52, v44, v45\n\t"
      "v_cvt_pk_bf16_f32 v53, v46, v47\n\t"
      "v_cvt_pk_bf16_f32 v54, v48, v49\n\t"
      "v_cvt_pk_bf16_f32 v55, v50, v51\n\t"
      "buffer_store_dwordx4 v[52:55], v15, s[24:27], s28 offen offset:2112 nt\n\t"
      "v_pk_fma_f32 v[248:249], v[248:249], v[96:97], v[104:105]\n\t"
      "v_pk_fma_f32 v[250:251], v[250:251], v[98:99], v[106:107]\n\t"
      "v_pk_fma_f32 v[252:253], v[252:253], v[100:101], v[108:109]\n\t"
      "v_pk_fma_f32 v[254:255], v[254:255], v[102:103], v[110:111]\n\t"
      "v_pk_fma_f32 v[44:45], v[248:249], v[248:249], v[34:35] neg_lo:[1,0,0] neg_hi:[1,0,0]\n\t"
      "v_pk_fma_f32 v[46:47], v[250:251], v[250:251], v[34:35] neg_lo:[1,0,0] neg_hi:[1,0,0]\n\t"
      "v_pk_fma_f32 v[48:49], v[252:253], v[252:253], v[34:35] neg_lo:[1,0,0] neg_hi:[1,0,0]\n\t"
      "v_pk_fma_f32 v[50:51], v[254:255], v[254:255], v[34:35] neg_lo:[1,0,0] neg_hi:[1,0,0]\n\t"
      "v_pk_mul_f32 v[44:45], v[44:45], v[248:249]\n\t"
      "v_pk_mul_f32 v[46:47], v[46:47], v[250:251]\n\t"
      "v_pk_mul_f32 v[48:49], v[48:49], v[252:253]\n\t"
      "v_pk_mul_f32 v[50:51], v[50:51], v[254:255]\n\t"
      "v_exp_f32 v44, v44\n\t"
      "v_exp_f32 v45, v45\n\t"
      "v_exp_f32 v46, v46\n\t"
      "v_exp_f32 v47, v47\n\t"
      "v_exp_f32 v48, v48\n\t"
      "v_exp_f32 v49, v49\n\t"
      "v_exp_f32 v50, v50\n\t"
      "v_exp_f32 v51, v51\n\t"
      "v_pk_fma_f32 v[44:45], v[44:45], v[36:37], v[36:37]\n\t"
      "v_pk_fma_f32 v[46:47], v[46:47], v[36:37], v[36:37]\n\t"
      "v_pk_fma_f32 v[48:49], v[48:49], v[36:37], v[36:37]\n\t"
      "v_pk_fma_f32 v[50:51], v[50:51], v[36:37], v[36:37]\n\t"
      "v_rcp_f32 v44, v44\n\t"
      "v_rcp_f32 v45, v45\n\t"
      "v_rcp_f32 v46, v46\n\t"
      "v_rcp_f32 v47, v47\n\t"
      "v_rcp_f32 v48, v48\n\t"
      "v_rcp_f32 v49, v49\n\t"
      "v_rcp_f32 v50, v50\n\t"
      "v_rcp_f32 v51, v51\n\t"
      "v_pk_mul_f32 v[44:45], v[44:45], v[248:249]\n\t"
      "v_pk_mul_f32 v[46:47], v[46:47], v[250:251]\n\t"
      "v_pk_mul_f32 v[48:49], v[48:49], v[252:253]\n\t"
      "v_pk_mul_f32 v[50:51], v[50:51], v[254:255]\n\t"
      "v_cvt_pk_bf16_f32 v56, v44, v45\n\t"
      "v_cvt_pk_bf16_f32 v57, v46, v47\n\t"
      "v_cvt_pk_bf16_f32 v58, v48, v49\n\t"
      "v_cvt_pk_bf16_f32 v59, v50, v51\n\t"
      "buffer_store_dwordx4 v[56:59], v15, s[24:27], s28 offen offset:3136 nt\n\t"
      "conv_end:\n\t"
      :
      : "{s[0:1]}"(__builtin_amdgcn_kernarg_segment_ptr()), "{s2}"(blockIdx.x), "{v0}"(threadIdx.x), "{s64}"((unsigned)(size_t)(__attribute__((address_space(3))) char*)smem)
      : "memory", "scc", "vcc", "v1","v2","v3","v4","v5","v6","v7","v8","v9","v10","v11","v12","v13","v14","v15","v16","v17","v18","v19","v20","v21","v22","v23","v24","v25","v26","v27","v28","v29","v30","v31","v32","v33","v34","v35","v36","v37","v38","v39","v40","v41","v42","v43","v44","v45","v46","v47","v48","v49","v50","v51","v52","v53","v54","v55","v56","v57","v58","v59","v60","v61","v62","v63","v64","v65","v66","v67","v68","v69","v70","v71","v72","v73","v74","v75","v76","v77","v78","v79","v80","v81","v82","v83","v84","v85","v86","v87","v88","v89","v90","v91","v92","v93","v94","v95","v96","v97","v98","v99","v100","v101","v102","v103","v104","v105","v106","v107","v108","v109","v110","v111","v112","v113","v114","v115","v116","v117","v118","v119","v120","v121","v122","v123","v124","v125","v126","v127","v128","v129","v130","v131","v132","v133","v134","v135","v136","v137","v138","v139","v140","v141","v142","v143","v144","v145","v146","v147","v148","v149","v150","v151","v152","v153","v154","v155","v156","v157","v158","v159","v160","v161","v162","v163","v164","v165","v166","v167","v168","v169","v170","v171","v172","v173","v174","v175","v176","v177","v178","v179","v180","v181","v182","v183","v184","v185","v186","v187","v188","v189","v190","v191","v192","v193","v194","v195","v196","v197","v198","v199","v200","v201","v202","v203","v204","v205","v206","v207","v208","v209","v210","v211","v212","v213","v214","v215","v216","v217","v218","v219","v220","v221","v222","v223","v224","v225","v226","v227","v228","v229","v230","v231","v232","v233","v234","v235","v236","v237","v238","v239","v240","v241","v242","v243","v244","v245","v246","v247","v248","v249","v250","v251","v252","v253","v254","v255","s3","s4","s5","s6","s7","s8","s9","s10","s11","s12","s13","s14","s15","s16","s17","s18","s19","s20","s21","s22","s23","s24","s25","s26","s27","s28","s29","s30","s31","s33","s34","s35","s36","s37","s38","s39","s40","s41","s42","s43","s44","s45","s46","s47","s48","s49","s50","s51","s52","s53","s54","s55","s56","s57","s58","s59","s60","s61","s62","s63");
}

extern "C" void kernel_launch(void* const* d_in, const int* in_sizes, int n_in, void* d_out, int out_size, void* d_ws, size_t ws_size, hipStream_t stream) {
  conv_stem_kernel<<<256, 512, 0, stream>>>(d_in[0], d_in[1], d_in[2], d_in[3], d_out, d_ws);
}
```
